```python
import torch

def _softmax(x, axis=-1):
    return torch.softmax(x, dim=axis)

def _gelu(x):
    return torch.nn.functional.gelu(x, approximate='tanh')

def _silu(x):
    return torch.nn.functional.silu(x)

def _sigmoid(x):
    return torch.sigmoid(x)

def _layernorm(x, axis=-1, eps=1e-05):
    m = x.mean(dim=axis, keepdim=True)
    v = x.var(dim=axis, keepdim=True, unbiased=False)
    return (x - m) / torch.sqrt(v + eps)

def _scatter_add(tgt, idx, src, axis=0):
    return tgt.clone().index_add_(axis, idx, src)

def _segment_sum(src, seg, n):
    out = torch.zeros((n,) + src.shape[1:], dtype=src.dtype)
    return out.index_add_(0, seg, src)

def _pad(x, pw):
    flat = tuple((v for pair in reversed(pw) for v in pair))
    return torch.nn.functional.pad(x, flat)
kernel_name = 'adaLN_cond__linear_kernel_hedgehog__geglu_ffn__prompt'

def setup_inputs():

    _w = lambda a: (lambda t: t.to(torch.bfloat16) if t.is_floating_point() else t)(torch.as_tensor(a))

    def _A_setup():
        B, S, D, H = (1, 2048, 640, 32)
        Dh = D // H
        return {'x': _w((lambda *s: torch.randn(*s, dtype=torch.bfloat16) * 0.05)(B, S, D)), 'wqkv': _w((lambda *s: torch.randn(*s, dtype=torch.bfloat16) * 0.4)(D, 3 * D)), 'w_phi': _w((lambda *s: torch.randn(*s, dtype=torch.bfloat16) * 0.4)(H, Dh, Dh) * Dh ** (-0.5)), 'wo': _w((lambda *s: torch.randn(*s, dtype=torch.bfloat16) * 0.05)(D, D))}
    _A_in = _A_setup()

    def _B_setup():
        B, S, D, F = (1, 2048, 640, 1728)
        return {'x': _w((lambda *s: torch.randn(*s, dtype=torch.bfloat16) * 0.05)(B, S, D)), 'w_gate': _w((lambda *s: torch.randn(*s, dtype=torch.bfloat16) * 0.05)(D, F)), 'w_up': _w((lambda *s: torch.randn(*s, dtype=torch.bfloat16) * 0.05)(D, F)), 'w_down': _w((lambda *s: torch.randn(*s, dtype=torch.bfloat16) * 0.05)(F, D))}
    _B_in = _B_setup()
    return {'A_x': _A_in['x'], 'A_wqkv': _A_in['wqkv'], 'A_w_phi': _A_in['w_phi'], 'A_wo': _A_in['wo'], 'B_x': _B_in['x'], 'B_w_gate': _B_in['w_gate'], 'B_w_up': _B_in['w_up'], 'B_w_down': _B_in['w_down'], 'c': _w((lambda *s: torch.randn(*s, dtype=torch.bfloat16) * 0.1)(1, 640) * 8.0), 'w_mod': _w((lambda *s: torch.randn(*s, dtype=torch.bfloat16) * 0.1)(640, 6 * 640) * 640 ** (-0.5) * 8.0)}

def reference(A_x, A_wqkv, A_w_phi, A_wo, B_x, B_w_gate, B_w_up, B_w_down, c, w_mod):

    def _A_ref(x, wqkv, w_phi, wo):
        B, S, D = x.shape
        H = 32
        Dh = D // H
        qkv = (lambda x, *d: x.permute(*d))((x @ wqkv).reshape(B, S, 3, H, Dh), 2, 0, 3, 1, 4)
        q, k, v = (qkv[0], qkv[1], qkv[2])
        pq = torch.einsum('bhsd,hdf->bhsf', q, w_phi)
        pk = torch.einsum('bhsd,hdf->bhsf', k, w_phi)
        phi_q = torch.concatenate([_softmax(pq, axis=-1), _softmax(-pq, axis=-1)], axis=-1)
        phi_k = torch.concatenate([_softmax(pk, axis=-1), _softmax(-pk, axis=-1)], axis=-1)
        kv = torch.einsum('bhsf,bhsd->bhfd', phi_k, v)
        z = torch.einsum('bhsf->bhf', phi_k)
        num = torch.einsum('bhqf,bhfd->bhqd', phi_q, kv)
        den = torch.einsum('bhqf,bhf->bhq', phi_q, z)[..., None] + 1e-06
        o = num / den
        return (lambda x, *d: x.permute(*d))(o, 0, 2, 1, 3).reshape(B, S, D) @ wo

    def _B_ref(x, w_gate, w_up, w_down):
        return _gelu(x @ w_gate) * (x @ w_up) @ w_down
    x = A_x
    mod = (_silu(c) @ w_mod).reshape(c.shape[0], 6, 1, -1)
    sa, ca, ga, sb, cb, gb = (mod[:, 0].clone(), mod[:, 1].clone(), mod[:, 2].clone(), mod[:, 3].clone(), mod[:, 4].clone(), mod[:, 5].clone())
    h = _layernorm(x, axis=-1) * (1 + ca) + sa
    x = x + ga * _A_ref(h, A_wqkv, A_w_phi, A_wo)
    h = _layernorm(x, axis=-1) * (1 + cb) + sb
    x = x + gb * _B_ref(h, B_w_gate, B_w_up, B_w_down)
    return x
```

```cpp
#include <hip/hip_runtime.h>
#include <hip/hip_bf16.h>
#include <stdint.h>

typedef unsigned short u16;
__device__ __forceinline__ float bf2f(u16 b) { return __uint_as_float(((unsigned)b) << 16); }
__device__ __forceinline__ u16 f2bf(float f) { unsigned u = __float_as_uint(f); u += 0x7fffu + ((u >> 16) & 1u); return (u16)(u >> 16); }

constexpr int S = 2048, D = 640, H = 32, DH = 20, F = 1728;

__global__ void k_mod(const u16* c, const u16* wmod, float* mod) {
  int n = blockIdx.x * blockDim.x + threadIdx.x;
  if (n >= 6 * D) return;
  float acc = 0.f;
  for (int k = 0; k < D; ++k) { float cv = bf2f(c[k]); float s = cv / (1.f + __expf(-cv)); acc += s * bf2f(wmod[(size_t)k * 6 * D + n]); }
  mod[n] = acc;
}
__global__ void k_ln(const float* x, const float* shift, const float* scale, float* h) {
  int r = blockIdx.x; int t = threadIdx.x;
  __shared__ float red[64];
  float s = 0.f; for (int k = t; k < D; k += 64) s += x[(size_t)r * D + k];
  for (int o = 32; o; o >>= 1) s += __shfl_xor(s, o);
  float m = s / D; float v = 0.f;
  for (int k = t; k < D; k += 64) { float d = x[(size_t)r * D + k] - m; v += d * d; }
  for (int o = 32; o; o >>= 1) v += __shfl_xor(v, o);
  float rs = rsqrtf(v / D + 1e-5f);
  for (int k = t; k < D; k += 64) h[(size_t)r * D + k] = bf2f(f2bf((x[(size_t)r * D + k] - m) * rs * (1.f + scale[k]) + shift[k]));
}
__global__ void k_cvt(const u16* x, float* y, int n) { int i = blockIdx.x * blockDim.x + threadIdx.x; if (i < n) y[i] = bf2f(x[i]); }
__global__ void k_gemm(const float* A, const u16* W, float* C, int M, int N, int K) {
  int n = blockIdx.x * blockDim.x + threadIdx.x; int m = blockIdx.y;
  if (n >= N) return;
  float acc = 0.f;
  for (int k = 0; k < K; ++k) acc += A[(size_t)m * K + k] * bf2f(W[(size_t)k * N + n]);
  C[(size_t)m * N + n] = acc;
}
__global__ void k_phi(const float* qkv, const u16* wphi, float* phiq, float* phik) {
  int idx = blockIdx.x * blockDim.x + threadIdx.x;
  if (idx >= S * H * 2) return;
  int which = idx & 1; int h = (idx >> 1) % H; int s = idx / (2 * H);
  const float* src = qkv + (size_t)s * 3 * D + which * D + h * DH;
  float* dst = (which ? phik : phiq) + ((size_t)s * H + h) * 40;
  float mx = -1e30f, mn = 1e30f;
  for (int f = 0; f < DH; ++f) { float a = 0.f; for (int d = 0; d < DH; ++d) a += src[d] * bf2f(wphi[(h * DH + d) * DH + f]); dst[f] = a; mx = fmaxf(mx, a); mn = fminf(mn, a); }
  float sp = 0.f, sn = 0.f;
  for (int f = 0; f < DH; ++f) { float a = dst[f]; float ep = __expf(a - mx), en = __expf(mn - a); dst[f] = ep; dst[20 + f] = en; sp += ep; sn += en; }
  for (int f = 0; f < DH; ++f) { dst[f] /= sp; dst[20 + f] /= sn; }
}
__global__ void k_kv(const float* qkv, const float* phik, float* kv) {
  int h = blockIdx.x; int t = threadIdx.x; if (t >= 840) return;
  int f = t / 21, d = t % 21;
  float acc = 0.f;
  for (int s = 0; s < S; ++s) { float p = phik[((size_t)s * H + h) * 40 + f]; float v = d < 20 ? qkv[(size_t)s * 3 * D + 2 * D + h * DH + d] : 1.f; acc += p * v; }
  kv[h * 840 + t] = acc;
}
__global__ void k_o(const float* phiq, const float* kv, float* o) {
  int idx = blockIdx.x * blockDim.x + threadIdx.x; if (idx >= S * D) return;
  int s = idx / D, c = idx % D, h = c / DH, d = c % DH;
  const float* p = phiq + ((size_t)s * H + h) * 40;
  float num = 0.f, den = 0.f;
  for (int f = 0; f < 40; ++f) { num += p[f] * kv[h * 840 + f * 21 + d]; den += p[f] * kv[h * 840 + f * 21 + 20]; }
  o[idx] = num / (den + 1e-6f);
}
__global__ void k_res(const float* x, const float* g, const float* y, float* out) {
  int idx = blockIdx.x * blockDim.x + threadIdx.x; if (idx >= S * D) return;
  out[idx] = x[idx] + g[idx % D] * y[idx];
}
__global__ void k_geglu(const float* g, const float* u, float* a) {
  int idx = blockIdx.x * blockDim.x + threadIdx.x; if (idx >= S * F) return;
  float x = g[idx]; float t = tanhf(0.7978845608028654f * (x + 0.044715f * x * x * x));
  a[idx] = 0.5f * x * (1.f + t) * u[idx];
}
__global__ void k_out(const float* x, const float* g, const float* y, u16* out) {
  int idx = blockIdx.x * blockDim.x + threadIdx.x; if (idx >= S * D) return;
  out[idx] = f2bf(x[idx] + g[idx % D] * y[idx]);
}

extern "C" void kernel_launch(void* const* d_in, const int* in_sizes, int n_in, void* d_out, int out_size, void* d_ws, size_t ws_size, hipStream_t stream) {
  const u16* x = (const u16*)d_in[0]; const u16* wqkv = (const u16*)d_in[1]; const u16* wphi = (const u16*)d_in[2]; const u16* wo = (const u16*)d_in[3];
  const u16* wg = (const u16*)d_in[5]; const u16* wu = (const u16*)d_in[6]; const u16* wd = (const u16*)d_in[7]; const u16* c = (const u16*)d_in[8]; const u16* wmod = (const u16*)d_in[9];
  float* ws = (float*)d_ws;
  float* mod = ws; ws += 4096;
  float* xf = ws; ws += S * D;
  float* h = ws; ws += S * D;
  float* qkv = ws; ws += S * 3 * D;
  float* phiq = ws; ws += S * H * 40;
  float* phik = ws; ws += S * H * 40;
  float* kv = ws; ws += H * 840 + 64;
  float* o = ws; ws += S * D;
  float* att = ws; ws += S * D;
  float* x1 = ws; ws += S * D;
  float* gg = ws; ws += S * F;
  float* uu = ws; ws += S * F;
  float* act = ws; ws += S * F;
  float* ffn = ws; ws += S * D;
  k_mod<<<(6 * D + 63) / 64, 64, 0, stream>>>(c, wmod, mod);
  k_cvt<<<S * D / 256, 256, 0, stream>>>(x, xf, S * D);
  k_ln<<<S, 64, 0, stream>>>(xf, mod, mod + D, h);
  k_gemm<<<dim3((3 * D + 127) / 128, S), 128, 0, stream>>>(h, wqkv, qkv, S, 3 * D, D);
  k_phi<<<S * H * 2 / 256, 256, 0, stream>>>(qkv, wphi, phiq, phik);
  k_kv<<<H, 1024, 0, stream>>>(qkv, phik, kv);
  k_o<<<S * D / 256, 256, 0, stream>>>(phiq, kv, o);
  k_gemm<<<dim3((D + 127) / 128, S), 128, 0, stream>>>(o, wo, att, S, D, D);
  k_res<<<S * D / 256, 256, 0, stream>>>(xf, mod + 2 * D, att, x1);
  k_ln<<<S, 64, 0, stream>>>(x1, mod + 3 * D, mod + 4 * D, h);
  k_gemm<<<dim3((F + 127) / 128, S), 128, 0, stream>>>(h, wg, gg, S, F, D);
  k_gemm<<<dim3((F + 127) / 128, S), 128, 0, stream>>>(h, wu, uu, S, F, D);
  k_geglu<<<S * F / 256, 256, 0, stream>>>(gg, uu, act);
  k_gemm<<<dim3((D + 127) / 128, S), 128, 0, stream>>>(act, wd, ffn, S, D, F);
  k_out<<<S * D / 256, 256, 0, stream>>>(x1, mod + 5 * D, ffn, (u16*)d_out);
}
```

```cpp
#include <hip/hip_runtime.h>
#include <stdint.h>

typedef unsigned short u16;
typedef unsigned u32;
typedef unsigned long long u64;
typedef u32 v4u __attribute__((ext_vector_type(4)));
typedef u32 v2u __attribute__((ext_vector_type(2)));
typedef float v4f __attribute__((ext_vector_type(4)));
typedef float v2f __attribute__((ext_vector_type(2)));
typedef float v16f __attribute__((ext_vector_type(16)));
typedef short v4s __attribute__((ext_vector_type(4)));
typedef __bf16 v8bf __attribute__((ext_vector_type(8)));
typedef __bf16 v2bf __attribute__((ext_vector_type(2)));
typedef _Float16 v2h __attribute__((ext_vector_type(2)));
#define LAS __attribute__((address_space(3)))
#define GAS __attribute__((address_space(1)))
#define DI __device__ __forceinline__
#define RLX_AGENT __ATOMIC_RELAXED, __HIP_MEMORY_SCOPE_AGENT
#define SB() __builtin_amdgcn_sched_barrier(0)
typedef __amdgpu_buffer_rsrc_t rsrc_t;

#define ROT_ON 1
namespace {
constexpr int SEQ = 2048, DM = 640, FF = 1728;
constexpr u32 WS_EPOCH = 0;
constexpr u32 WS_FLAG = 1024;
constexpr u32 WS_MODG = 32768;
constexpr u32 WS_AB = 49152;
constexpr u32 WS_KVP = WS_AB + 4 * 3456 * 16;
constexpr u32 KVP_STRIDE = 6784;
constexpr u32 WS_STAT = WS_KVP + 256 * KVP_STRIDE;
constexpr u32 WS_O = WS_STAT + SEQ * 64;
constexpr u32 WS_U = WS_O + SEQ * DM * 2;
constexpr u32 WS_ACT = WS_U + SEQ * DM * 2;
constexpr u32 WS_GU = WS_ACT + SEQ * FF * 2;
constexpr u32 WS_WD = WS_GU + 16 * 286720;
constexpr u32 WS_WQ = WS_WD + 8 * 276480;
constexpr u32 WS_WO = WS_WQ + 16 * 153600;
constexpr u32 WS_T = WS_WO + 16 * 102400;
constexpr u32 WS_END = WS_T + SEQ * DM * 2;
constexpr u32 WS_DBG = (WS_END + 4095) & ~4095u;
static_assert(WS_KVP % 128 == 0 && WS_STAT % 128 == 0 && WS_O % 256 == 0, "align");
constexpr u32 L_RQ = 102400;
constexpr u32 L_XS = 102400;
constexpr u32 L_XST = L_XS + 32768;
constexpr u32 L_TB = 0;
constexpr u32 L_EX = 49152;
constexpr u32 L_MISC = 153664;
constexpr u32 L_COA = L_MISC, L_COS = L_MISC + 2560;
constexpr u32 L_KVB = L_MISC + 5120;
constexpr u32 L_ZERO = L_KVB + 3360;
constexpr u32 L_WPHI = L_ZERO + 16;
constexpr u32 L_TOTAL = 163840;
constexpr u32 L_G2A = L_COA, L_G2S = L_COA + 640;
constexpr u32 L_G2R = L_COA + 1280;
constexpr u32 L_AB = L_COA + 3328;
static_assert(L_WPHI + 1600 <= L_TOTAL && L_AB + 1792 <= L_KVB, "lds");
constexpr u32 SPIN_CAP = 1u << 18;
constexpr int AUX_SC1 = 16;
constexpr u32 P3SLOT = 28800, P3UP = 14464;
constexpr u32 p3slot(int stage) { return ((stage + 4) % 5) * P3SLOT; }

DI float bflo(u32 v) { return __uint_as_float(v << 16); }
DI float bfhi(u32 v) { return __uint_as_float(v & 0xffff0000u); }
DI u32 pkbf(float a, float b) { v2f t = {a, b}; return __builtin_bit_cast(u32, __builtin_convertvector(t, v2bf)); }
DI float rbf(float a) { return bflo(pkbf(a, a)); }
DI void bar() { asm volatile("s_waitcnt lgkmcnt(0)\n\ts_barrier" ::: "memory"); }
DI void rawbar() { asm volatile("s_barrier" ::: "memory"); }
DI void drain() { asm volatile("s_waitcnt vmcnt(0)" ::: "memory"); }
template <int N> DI void waitv() { asm volatile("s_waitcnt vmcnt(%0)" ::"i"(N) : "memory"); }
template <int I> struct IC { static constexpr int v = I; };
template <int B, int E, class F> DI void sfor(F&& f) { if constexpr (B < E) { f(IC<B>{}); sfor<B + 1, E>(f); } }
DI rsrc_t mkrs(const void* p, u32 bytes) { return __builtin_amdgcn_make_buffer_rsrc((void*)p, 0, bytes, 0x00020000); }
DI void dma16(rsrc_t rs, u32 voff, u32 soff, u32 lds) {
  asm volatile("s_mov_b32 m0, %0\n\ts_nop 0\n\tbuffer_load_dwordx4 %1, %2, %3 offen lds" ::"s"(lds), "v"(voff), "s"(rs), "s"(soff) : "memory");
}
template <int NL> DI void dmam(rsrc_t rs, u32 voff, u32 soff, u32 lds) {
  asm volatile("s_mov_b32 m0, %0\n\ts_mov_b32 exec_hi, %4\n\tbuffer_load_dwordx4 %1, %2, %3 offen lds\n\ts_mov_b32 exec_hi, -1"
               ::"s"(lds), "v"(voff), "s"(rs), "s"(soff), "i"((1u << (NL - 32)) - 1u) : "memory");
}
DI void dma16c(rsrc_t rs, u32 voff, u32 soff, u32 lds) {
  asm volatile("s_mov_b32 m0, %0\n\ts_nop 0\n\tbuffer_load_dwordx4 %1, %2, %3 offen sc1 lds" ::"s"(lds), "v"(voff), "s"(rs), "s"(soff) : "memory");
}
template <int NL> DI void dmamc(rsrc_t rs, u32 voff, u32 soff, u32 lds) {
  asm volatile("s_mov_b32 m0, %0\n\ts_mov_b32 exec_hi, %4\n\tbuffer_load_dwordx4 %1, %2, %3 offen sc1 lds\n\ts_mov_b32 exec_hi, -1"
               ::"s"(lds), "v"(voff), "s"(rs), "s"(soff), "i"((1u << (NL - 32)) - 1u) : "memory");
}
DI u32 opq(u32 v) { asm volatile("" : "+v"(v)); return v; }
DI v4s trd(u32 addr) { return __builtin_amdgcn_ds_read_tr16_b64_v4i16((LAS v4s*)(size_t)addr); }
DI v8bf trfrag(u32 a0, u32 a1) {
  v4s a = trd(a0), b = trd(a1);
  return __builtin_bit_cast(v8bf, __builtin_shufflevector(a, b, 0, 1, 2, 3, 4, 5, 6, 7));
}
DI v16f mfma32(v8bf a, v8bf b, v16f c) { return __builtin_amdgcn_mfma_f32_32x32x16_bf16(a, b, c, 0, 0, 0); }
DI v4f mfma16(v8bf a, v8bf b, v4f c) { return __builtin_amdgcn_mfma_f32_16x16x32_bf16(a, b, c, 0, 0, 0); }
DI v8bf asbf(v4u v) { return __builtin_bit_cast(v8bf, v); }
DI v4u ldsr128(u32 a) { return *(LAS v4u*)(size_t)a; }
DI v4f ldsr128f(u32 a) { return *(LAS v4f*)(size_t)a; }
DI v2u ldsr64(u32 a) { return *(LAS v2u*)(size_t)a; }
DI void ldsw128(u32 a, v4u v) { *(LAS v4u*)(size_t)a = v; }
DI void ldsw128f(u32 a, v4f v) { *(LAS v4f*)(size_t)a = v; }
DI void ldsw64(u32 a, v2u v) { *(LAS v2u*)(size_t)a = v; }
DI float ldsrf(u32 a) { return *(LAS float*)(size_t)a; }
DI void ldswf(u32 a, float v) { *(LAS float*)(size_t)a = v; }
DI float mixlo(u32 t, float a, float s) { float r; asm("v_fma_mix_f32 %0, %1, %2, %3 op_sel_hi:[1,0,0]" : "=v"(r) : "v"(t), "v"(a), "v"(s)); return r; }
DI float mixhi(u32 t, float a, float s) { float r; asm("v_fma_mix_f32 %0, %1, %2, %3 op_sel:[1,0,0] op_sel_hi:[1,0,0]" : "=v"(r) : "v"(t), "v"(a), "v"(s)); return r; }
DI float xsum16(float v) {
  auto r = __builtin_amdgcn_permlane16_swap(__float_as_uint(v), __float_as_uint(v), false, false);
  return __uint_as_float(r[0]) + __uint_as_float(r[1]);
}
DI float xsum32(float v) {
  auto r = __builtin_amdgcn_permlane32_swap(__float_as_uint(v), __float_as_uint(v), false, false);
  return __uint_as_float(r[0]) + __uint_as_float(r[1]);
}
template <int CTRL> DI float dppadd(float v) {
  return v + __uint_as_float(__builtin_amdgcn_update_dpp(0u, __float_as_uint(v), CTRL, 0xf, 0xf, true));
}
constexpr int DPP_ROR4 = 0x124, DPP_ROR8 = 0x128;
DI v4u ldb128(rsrc_t rs, u32 vo, u32 so) { return __builtin_amdgcn_raw_buffer_load_b128(rs, vo, so, 0); }
DI v4u ldb128c(rsrc_t rs, u32 vo, u32 so) { return __builtin_amdgcn_raw_buffer_load_b128(rs, vo, so, AUX_SC1); }
template <int OFF> DI void aload(v4u& r, rsrc_t rs, u32 vo, u32 so) {
  asm volatile("buffer_load_dwordx4 %0, %1, %2, %3 offen offset:%4" : "=v"(r) : "v"(vo), "s"(rs), "s"(so), "i"(OFF) : "memory");
}
template <int OFF> DI void aloadc(v4u& r, rsrc_t rs, u32 vo, u32 so) {
  asm volatile("buffer_load_dwordx4 %0, %1, %2, %3 offen offset:%4 sc1" : "=v"(r) : "v"(vo), "s"(rs), "s"(so), "i"(OFF) : "memory");
}
template <int N> DI void await1(v4u& a) { asm volatile("s_waitcnt vmcnt(%1)" : "+v"(a) : "i"(N) : "memory"); }
template <int N> DI void await2(v4u& a, v4u& b) { asm volatile("s_waitcnt vmcnt(%2)" : "+v"(a), "+v"(b) : "i"(N) : "memory"); }
template <int N> DI void await4(v4u& a, v4u& b, v4u& c, v4u& d) { asm volatile("s_waitcnt vmcnt(%4)" : "+v"(a), "+v"(b), "+v"(c), "+v"(d) : "i"(N) : "memory"); }
DI void astorec(v4u v, rsrc_t rs, u32 vo, u32 so) { __builtin_amdgcn_raw_buffer_store_b128(v, rs, vo, so, AUX_SC1); }
template <int N> DI void await3(v4u& a, v4u& b, v4u& c) { asm volatile("s_waitcnt vmcnt(%3)" : "+v"(a), "+v"(b), "+v"(c) : "i"(N) : "memory"); }
DI void stb128c(rsrc_t rs, u32 vo, v4u v) { __builtin_amdgcn_raw_buffer_store_b128(v, rs, vo, 0, AUX_SC1); }
DI void stb64c(rsrc_t rs, u32 vo, v2u v) { __builtin_amdgcn_raw_buffer_store_b64(v, rs, vo, 0, AUX_SC1); }

#define WST(i) do {} while (0)
#define STAMP(i) do {} while (0)

DI void publish(char* ws, u32 flagoff, u32 tag) {
  drain();
  bar();
  if (threadIdx.x == 0) __hip_atomic_store((GAS u32*)(ws + flagoff), tag, RLX_AGENT);
}
DI void wait_line(char* ws, u32 lineoff, u32 tag, int wave, int lane, int mask) {
  if (wave == 0) {
    GAS u32* f = (GAS u32*)(ws + lineoff) + (lane & mask);
#pragma unroll 1
    for (u32 spin = 0; spin < SPIN_CAP; ++spin) {
      const u32 v = __hip_atomic_load(f, RLX_AGENT);
      if (__all(v == tag)) break;
      __builtin_amdgcn_s_sleep(1);
    }
  }
  bar();
}
DI void poll_line(char* ws, u32 off, u32 tag) {
  GAS u32* f = (GAS u32*)(ws + off);
#pragma unroll 1
  for (u32 spin = 0; spin < SPIN_CAP; ++spin) {
    const u32 v = __hip_atomic_load(f, RLX_AGENT);
    if (__all(v == tag)) break;
    __builtin_amdgcn_s_sleep(1);
  }
}
DI void gather2(rsrc_t rs, u32 o1, u32 o2, u32 tag, v4u& g1, v4u& g2) {
#pragma unroll 1
  for (u32 spin = 0; spin < SPIN_CAP; ++spin) {
    asm volatile("" ::: "memory");
    g1 = ldb128c(rs, o1, 0);
    g2 = ldb128c(rs, o2, 0);
    if (__all(g1[1] == tag && g1[3] == tag && g2[1] == tag && g2[3] == tag)) break;
    __builtin_amdgcn_s_sleep(1);
  }
}
DI void gather4(rsrc_t rs, u32 o, u32 st, u32 tag, v4u& g0, v4u& g1, v4u& g2, v4u& g3) {
#pragma unroll 1
  for (u32 spin = 0; spin < SPIN_CAP; ++spin) {
    asm volatile("" ::: "memory");
    g0 = ldb128c(rs, o, 0); g1 = ldb128c(rs, o, st); g2 = ldb128c(rs, o, 2 * st); g3 = ldb128c(rs, o, 3 * st);
    if (__all(g0[1] == tag && g0[3] == tag && g1[1] == tag && g1[3] == tag && g2[1] == tag && g2[3] == tag && g3[1] == tag && g3[3] == tag)) break;
    __builtin_amdgcn_s_sleep(1);
  }
}
DI v4f unp4(v4u g, float one) { return v4f{rbf(one + bflo(g[0])), rbf(one + bfhi(g[0])), rbf(one + bflo(g[2])), rbf(one + bfhi(g[2]))}; }

DI void softmax20(const float (&p)[20], u32 (&o)[10]) {
  float mx = p[0];
#pragma unroll
  for (int i = 1; i < 20; ++i) mx = fmaxf(mx, p[i]);
  const float L2E = 1.4426950408889634f, a = -mx * L2E;
  float e[20], s0 = 0.f, s1 = 0.f;
#pragma unroll
  for (int i = 0; i < 20; ++i) { e[i] = __builtin_amdgcn_exp2f(__builtin_fmaf(p[i], L2E, a)); if (i & 1) s1 += e[i]; else s0 += e[i]; }
  const float r = __builtin_amdgcn_rcpf(s0 + s1);
#pragma unroll
  for (int i = 0; i < 10; ++i) o[i] = pkbf(e[2 * i] * r, e[2 * i + 1] * r);
}
constexpr int WO_EARLY = 6;
constexpr u32 L_P3X = 40960;

__global__ __launch_bounds__(512, 2) void fused_kernel(const u16* __restrict__ x, const u16* __restrict__ wqkv, const u16* __restrict__ wphi,
                                                       const u16* __restrict__ wo, const u16* __restrict__ wg, const u16* __restrict__ wu,
                                                       const u16* __restrict__ wd, const u16* __restrict__ cvec, const u16* __restrict__ wmod,
                                                       u16* __restrict__ out, char* ws) {
  const int tid = threadIdx.x, lane = tid & 63;
  const int wave = __builtin_amdgcn_readfirstlane(tid >> 6);
  const int b = blockIdx.x, xcd = b & 7, idx = b >> 3;
  const int P = 4 * (xcd >> 1) + (idx >> 3);
  const int J = 8 * (xcd & 1) + (idx & 7);
  const int TT = wave >> 1, KH = wave & 1;
  const int R1 = ROT_ON * (((idx & 7) + (idx >> 3)) % 5), R3 = ROT_ON * ((((idx & 7) * 4 + (idx >> 3)) * 10) >> 5), R4 = ROT_ON * ((((idx & 7) * 4 + (idx >> 3)) * 27) >> 5);
  auto wrap = [](int v, int m) { return v >= m ? v - m : v; };
  STAMP(0);
  const rsrc_t rs_ws = mkrs(ws, WS_END);
  const rsrc_t rs_x = mkrs(x, SEQ * DM * 2);
  const rsrc_t rs_w = mkrs(wqkv, DM * 3 * DM * 2);
  const int hh = lane >> 5, i32 = lane & 31, g4 = lane >> 4, t16 = lane & 15;
  const int pg = ((g4 & 1) << 1) | (g4 >> 1);
  const int g2 = g4 & 1, q4 = t16 >> 2, p4 = lane & 3;

  constexpr u32 OOB = 0x7ffffff0u;
  const int TH = J >> 3, CT = J & 7;
  const int l10 = lane / 10, c10 = lane - l10 * 10;
  const u32 vlin = lane * 16;
  const int li = b >> 1;
  const bool domod = (b & 1) == 0 && li < 120;
  auto modtask = [&](u32 tg) {
    const rsrc_t rs_wm = mkrs(wmod, DM * 6 * DM * 2);
    const rsrc_t rs_c = mkrs(cvec, DM * 2);
    const u32 coff2 = (80 * wave + 5 * (lane >> 2)) * 2;
    const u32 vo = coff2 * 3840 + li * 64 + (lane & 3) * 16;
    v4u wm[5];
    const v4u cq = ldb128(rs_c, coff2 & ~3u, 0);
#pragma unroll
    for (int i = 0; i < 5; ++i) wm[i] = ldb128(rs_wm, vo, i * 7680);
    const u32 sh = (coff2 & 2) * 8;
    const u32 ce[3] = {__builtin_amdgcn_alignbit(cq[1], cq[0], sh), __builtin_amdgcn_alignbit(cq[2], cq[1], sh), cq[2] >> sh};
    float acc[8] = {0.f, 0.f, 0.f, 0.f, 0.f, 0.f, 0.f, 0.f};
#pragma unroll
    for (int i = 0; i < 5; ++i) {
      const float cv = (i & 1) ? bfhi(ce[i >> 1]) : bflo(ce[i >> 1]);
      const float sv = rbf(cv * __builtin_amdgcn_rcpf(1.f + __builtin_amdgcn_exp2f(-1.4426950408889634f * cv)));
#pragma unroll
      for (int e = 0; e < 4; ++e) { acc[2 * e] += sv * bflo(wm[i][e]); acc[2 * e + 1] += sv * bfhi(wm[i][e]); }
    }
#pragma unroll
    for (int e = 0; e < 8; ++e) acc[e] = xsum32(xsum16(dppadd<DPP_ROR8>(dppadd<DPP_ROR4>(acc[e]))));
    if (lane < 4) {
      ldsw128f(L_KVB + wave * 128 + lane * 32, v4f{acc[0], acc[1], acc[2], acc[3]});
      ldsw128f(L_KVB + wave * 128 + lane * 32 + 16, v4f{acc[4], acc[5], acc[6], acc[7]});
    }
    bar();
    if (tid < 16) {
      v2f sm = {0.f, 0.f};
#pragma unroll
      for (int w = 0; w < 8; ++w) sm += *(LAS v2f*)(size_t)(L_KVB + w * 128 + tid * 8);
      __hip_atomic_store((GAS u64*)(ws + WS_MODG) + li * 16 + tid, ((u64)tg << 32) | pkbf(sm[0], sm[1]), RLX_AGENT);
    }
  };
  v4u tagq, xa, xb2, rw[2], ro, wpa, wpb;
  u32 rwd[2];
  aloadc<0>(tagq, rs_ws, WS_EPOCH, 0);
  {
    const u32 vx = lane < 40 ? (128 * P + 8 * J + wave) * 1280 + vlin : OOB;
    aload<0>(xa, rs_x, vx, 0); aload<640>(xb2, rs_x, vx, 0);
    const int r5 = lane / 5, c5 = lane - r5 * 5;
    sfor<0, 2>([&](auto I) {
      const int run = wave + 8 * I.v, rq = run / 3, part = run - rq * 3;
      const int pos = 40 * P + 10 * rq + r5;
      const int k = (pos & ~15) | ((pos & 3) << 2) | ((pos >> 2) & 3);
      const bool ok = lane < 50 && run < 12;
      aload<0>(rw[I.v], rs_w, ok ? k * 3840 + c5 * 16 : OOB, J * 80 + part * 1280);
      rwd[I.v] = ok ? (pos & 63) * 80 + c5 * 16 + (part == 0 ? 102400 + (pos >> 6) * 5120 : (pos >> 6) * 10240 + (part - 1) * 5120) : OOB;
    });
    const rsrc_t rs_wo = mkrs(wo, DM * DM * 2), rs_ph = mkrs(wphi, 32 * 400 * 2);
    const int pos = 40 * P + 5 * wave + l10;
    aload<0>(ro, rs_wo, lane < 50 ? ((pos & ~12) | ((pos & 4) << 1) | ((pos & 8) >> 1)) * 1280 + c10 * 16 : OOB, CT * 160);
    aload<0>(wpa, rs_ph, vlin, J * 1600);
    aload<1024>(wpb, rs_ph, (lane < 36 ? lane : 35) * 16, J * 1600);
  }
  SB();
  if (domod && li < 40) { await1<7>(tagq); modtask(tagq[0] + 1u); }
  WST(0);
  await3<5>(tagq, xa, xb2);
  const u32 tag = tagq[0] + 1u;
  {
    float v[16];
#pragma unroll
    for (int e = 0; e < 4; ++e) { v[2 * e] = bflo(xa[e]); v[2 * e + 1] = bfhi(xa[e]); v[8 + 2 * e] = bflo(xb2[e]); v[9 + 2 * e] = bfhi(xb2[e]); }
    auto wsum = [](float a) { return xsum32(xsum16(dppadd<DPP_ROR8>(dppadd<DPP_ROR4>(dppadd<0x4e>(dppadd<0xb1>(a)))))); };
    float sm = 0.f;
#pragma unroll
    for (int e = 0; e < 16; ++e) sm += v[e];
    const float m = wsum(sm) * (1.f / DM);
    float q = 0.f;
#pragma unroll
    for (int e = 0; e < 16; ++e) { v[e] -= m; q += v[e] * v[e]; }
    const float rs = __builtin_amdgcn_rsqf(wsum(lane < 40 ? q : 0.f) * (1.f / DM) + 1e-5f);
    auto pkh = [&](int e) { v2f t = {v[e] * rs, v[e + 1] * rs}; return __builtin_bit_cast(u32, __builtin_convertvector(t, v2h)); };
    if (lane < 40) {
      ldsw128(lane * 144 + wave * 16, v4u{pkh(0), pkh(2), pkh(4), pkh(6)});
      ldsw128((40 + lane) * 144 + wave * 16, v4u{pkh(8), pkh(10), pkh(12), pkh(14)});
    }
  }
  await3<2>(rw[0], rw[1], ro);
  astorec(rw[0], rs_ws, rwd[0], WS_WQ + J * 153600);
  astorec(rw[1], rs_ws, rwd[1], WS_WQ + J * 153600);
  astorec(ro, rs_ws, lane < 50 ? vlin : OOB, WS_WO + J * 102400 + (40 * P + 5 * wave) * 160);
  bar();
  {
    const u32 tb = WS_T + ((8 * P + (J >> 1)) * 80 + 10 * wave) * 256 + (J & 1) * 128;
    const int o8 = lane >> 3, tk = lane & 7;
    astorec(ldsr128((10 * wave + o8) * 144 + tk * 16), rs_ws, o8 * 256 + tk * 16, tb);
    if (lane < 16) astorec(ldsr128((10 * wave + 8 + o8) * 144 + tk * 16), rs_ws, (8 + o8) * 256 + tk * 16, tb);
  }
  WST(1);
  drain();
  asm volatile("" : "+v"(wpa), "+v"(wpb));
  ldsw128(L_WPHI + lane * 16, wpa);
  ldsw128(L_WPHI + 1024 + (lane < 36 ? lane : 35) * 16, wpb);
  ldsw128(L_ZERO, v4u{0u, 0u, 0u, 0u});
  bar();
  if (tid < 2) __hip_atomic_store((GAS u32*)(ws + WS_FLAG + (tid ? (5 * 16 + J) * 256 + P * 4 : (6 * 16 + P) * 256 + J * 4)), tag, RLX_AGENT);
  STAMP(1);
  WST(2);
  if (domod && li >= 40) modtask(tag);
  const int t0 = 7 * J - (J >> 2), nt = (J & 3) == 3 ? 6 : 7;
  const int rq27 = 2 * P + TH;
  auto kgrp = [](int gp) { return (gp & ~3) | ((gp & 1) << 1) | ((gp >> 1) & 1); };
  v4u wpf[2][3];
  {
    const bool act = ((i32 >> 2) & 1) == hh;
    const u32 sgn = KH ? 0x80008000u : 0u;
    const u32 m0 = act ? 0xffffffffu : 0u, m1 = (act && (i32 >> 3) == 0) ? 0xffffffffu : 0u;
    const u32 r0 = L_WPHI + hh * 800 + q4 * 40;
    const u32 rm = r0 + (hh ? 0 : 160), rl = r0 + (hh ? 640 : 0);
    const u32 c0 = (2 * g2 + (p4 >> 1) + KH) * 8, c1 = KH ? 0 : 32;
#pragma unroll
    for (int T = 0; T < 2; ++T) {
      const u32 c = T ? c1 : c0, mk = T ? m1 : m0;
      wpf[T][0] = __builtin_bit_cast(v4u, trfrag(rm + c, rm + c + 160));
      wpf[T][1] = __builtin_bit_cast(v4u, trfrag(rm + c + 320, rm + c + 480));
      const v2u l = __builtin_bit_cast(v2u, trd(rl + c));
      wpf[T][2] = v4u{l[0], l[1], 0u, 0u};
#pragma unroll
      for (int s = 0; s < 3; ++s) {
#pragma unroll
        for (int e = 0; e < (s < 2 ? 4 : 2); ++e) wpf[T][s][e] = (wpf[T][s][e] ^ sgn) & mk;
        asm volatile("" : "+v"(wpf[T][s]));
      }
    }
  }
  SB();
  WST(3);
  if (wave < 5) {
    const u32 mgq = 32 * wave + i32;
    const u32 mgo = WS_MODG + hh * 2560 + wrap(mgq + 32 * R1, 160) * 16;
    v4u mg;
#pragma unroll 1
    for (u32 spin = 0; spin < SPIN_CAP; ++spin) {
      asm volatile("" ::: "memory");
      mg = ldb128c(rs_ws, mgo, 0);
      if (__all(mg[1] == tag && mg[3] == tag)) break;
      __builtin_amdgcn_s_sleep(1);
    }
    ldsw128f((hh ? L_COA : L_COS) + mgq * 16, unp4(mg, hh ? 1.f : 0.f));
  } else if (wave == 7) {
    poll_line(ws, WS_FLAG + (hh ? (5 * 16 + J) * 256 : (6 * 16 + P) * 256) + t16 * 4, tag);
  }
  WST(4);
  bar();
  STAMP(2);
  v4u xf[2][10];
  const u32 wqs = WS_WQ + J * 153600;
  const u32 s12 = __builtin_amdgcn_readfirstlane(wave < 4 ? wrap(96 + wave + 20 * R1, 100) : 100 + wrap(wave - 4 + 10 * R1, 50));
  const int w18 = wave < 6 ? wave : 5;
  auto wq1 = [&](int z) {
    const int w = z == 18 ? w18 : wave;
    const u32 src = z < 12 ? wrap(8 * z + w + 20 * R1, 100) : z == 12 ? s12 : 100 + wrap(8 * z - 100 + w + 10 * R1, 50);
    dma16c(rs_ws, vlin, wqs + src * 1024, (8 * z + w) * 1024);
  };
  const u32 tvo = WS_T + (8 * P + 2 * TT) * 20480 + KH * 2048 + pg * 256 + t16 * 16;
  auto ldt = [&](auto N) { constexpr int n = N.v; const u32 so = wrap((n >> 1) + R1, 5) * 4096;
    aloadc<(n & 1) * 1024>(xf[0][n], rs_ws, tvo, so); aloadc<(n & 1) * 1024>(xf[1][n], rs_ws, tvo, so + 20480); };
  wq1(0); wq1(1); wq1(2); ldt(IC<0>{}); ldt(IC<1>{}); wq1(3); wq1(4); ldt(IC<2>{}); ldt(IC<3>{});
  SB();
  await2<8>(xf[0][0], xf[1][0]);
  rawbar();
  STAMP(3);
  v16f aK = {}, aV = {};
  v4f lk, lv;
  const u32 rowb80 = (4 * q4 + 2 * hh) * 80;
  const u32 bmain = rowb80 + ((p4 & 1) ? 40 : 8) + 8 * (2 * g2 + (p4 >> 1));
  {
    u32 bt[2], blz[2];
    bt[0] = opq(bmain + KH * 10240); bt[1] = opq(bmain + KH * 10240 + 61440);
    blz[0] = opq((p4 & 1) * 5120 + ((p4 >> 1) ? 72 : 0) + KH * 10240 + (16 * (pg >> 1) + 4 * q4 + 2 * (pg & 1)) * 80); blz[1] = opq(blz[0] + 61440);
    auto wa = [&](int n, int f) { return bt[n >= 6] + ((n >> 1) % 3) * 20480 + (n & 1) * 2560 + (f >> 1) * 1280 + (f & 1) * 5120; };
    auto la = [&](int n) { return blz[n >= 6] + ((n >> 1) % 3) * 20480 + (n & 1) * 2560; };
    const u32 cba = opq(L_COA + pg * 32 + KH * 256);
    constexpr auto coff = [](int n) { return (u32)((n >> 1) * 512 + (n & 1) * 128); };
    v8bf wf[4], wl;
    v4f A0, A1, S0, S1, l0 = {}, l1 = {};
    auto mod2 = [&](int T, int s, int e0, const v4f& A, const v4f& S) {
      const u32 t0 = xf[T][s][e0], t1 = xf[T][s][e0 + 1];
      const float a = mixlo(t0, A[0], S[0]), b2 = mixhi(t0, A[1], S[1]), c = mixlo(t1, A[2], S[2]), d = mixhi(t1, A[3], S[3]);
      xf[T][s][e0] = pkbf(a, b2); xf[T][s][e0 + 1] = pkbf(c, d);
    };
    auto swp = [&](int s, int e) { auto r = __builtin_amdgcn_permlane16_swap(xf[0][s][e], xf[1][s][e], false, false); xf[0][s][e] = r[0]; xf[1][s][e] = r[1]; };
    A0 = ldsr128f(cba); A1 = ldsr128f(cba + 16); S0 = ldsr128f(cba + 2560); S1 = ldsr128f(cba + 2576);
    wl = trfrag(la(0), la(0) + 80);
#pragma unroll
    for (int f = 0; f < 4; ++f) wf[f] = trfrag(wa(0, f), wa(0, f) + 80);
    mod2(0, 0, 0, A0, S0); mod2(0, 0, 2, A1, S1); mod2(1, 0, 0, A0, S0); mod2(1, 0, 2, A1, S1);
    A0 = ldsr128f(cba + 128); A1 = ldsr128f(cba + 144); S0 = ldsr128f(cba + 2688); S1 = ldsr128f(cba + 2704);
    l0 = mfma16(wl, asbf(xf[0][0]), l0); l1 = mfma16(wl, asbf(xf[1][0]), l1);
    wl = trfrag(la(1), la(1) + 80);
    swp(0, 0); swp(0, 1); swp(0, 2); swp(0, 3);
    SB();
    sfor<0, 10>([&](auto N) {
      constexpr int n = N.v;
      constexpr auto nw = [](int m) { return m < 4 ? 2 : 1; };
      constexpr auto no = [=](int m) { return nw(m) + (m < 6 ? 2 : 0); };
      constexpr auto sum = [=](int a, int b2) { int c = 0; for (int m = a; m < b2; ++m) c += no(m); return c; };
      if constexpr (n < 9) {
        constexpr int c = n + 1 < 4 ? (n + 1 == 1 ? 6 : n + 1 == 2 ? 2 : 0) + sum(0, n) : sum(n + 1 - 4 + 1, n);
        await2<c>(xf[0][n + 1], xf[1][n + 1]);
      }
      if constexpr (n == 0) { waitv<4>(); rawbar(); }
      if constexpr (n == 2) { waitv<1 + 2>(); rawbar(); }
      if constexpr (n == 4) { waitv<1 + 2 + no(3)>(); rawbar(); }
      if constexpr (n == 6) { waitv<2 + no(4) + no(5)>(); rawbar(); }
      SB();
      sfor<0, 4>([&](auto F) {
        constexpr int f = F.v;
        const v8bf hb = asbf(xf[f >> 1][n]);
        if (f & 1) aV = mfma32(wf[f], hb, aV); else aK = mfma32(wf[f], hb, aK);
        if constexpr (n < 9) {
          wf[f] = trfrag(wa(n + 1, f), wa(n + 1, f) + 80);
          if (f == 0) mod2(0, n + 1, 0, A0, S0);
          if (f == 1) mod2(0, n + 1, 2, A1, S1);
          if (f == 2) { mod2(1, n + 1, 0, A0, S0); if (n < 8) { A0 = ldsr128f(cba + coff(n + 2)); S0 = ldsr128f(cba + 2560 + coff(n + 2)); } }
          if (f == 3) { mod2(1, n + 1, 2, A1, S1); if (n < 8) { A1 = ldsr128f(cba + coff(n + 2) + 16); S1 = ldsr128f(cba + 2576 + coff(n + 2)); } }
        }
        if constexpr (f == 0) wq1(n < 4 ? 5 + 2 * n : 9 + n);
        if constexpr (f == 1 && n < 4) wq1(6 + 2 * n);
        if constexpr (f == 2 && n < 6) ldt(IC<(n < 6 ? n + 4 : 0)>{});
        SB();
      });
      if constexpr (n < 9) {
        l0 = mfma16(wl, asbf(xf[0][n + 1]), l0); l1 = mfma16(wl, asbf(xf[1][n + 1]), l1);
        if (n < 8) wl = trfrag(la(n + 2), la(n + 2) + 80);
        swp(n + 1, 0); swp(n + 1, 1); swp(n + 1, 2); swp(n + 1, 3);
      }
      SB();
    });
    asm volatile("" : "+v"(aK), "+v"(aV));
#pragma unroll
    for (int e = 0; e < 4; ++e) {
      auto r = __builtin_amdgcn_permlane16_swap(__float_as_uint(l0[e]), __float_as_uint(l1[e]), false, false);
      lk[e] = __uint_as_float(r[0]); lv[e] = __uint_as_float(r[1]);
    }
  }
  WST(5);
  STAMP(4);
  auto phi = [&](const v16f& m, float l0, float l1, float l2, float l3, u32 exb, u32 (&o)[10], auto&& extra) {
    const v4u b0 = {pkbf(m[0], m[1]), pkbf(m[2], m[3]), pkbf(m[4], m[5]), pkbf(m[6], m[7])};
    const v4u b1 = {pkbf(m[8], m[9]), pkbf(m[10], m[11]), pkbf(m[12], m[13]), pkbf(m[14], m[15])};
    const v4u b2 = {pkbf(l0, l1), pkbf(l2, l3), 0u, 0u};
    bar();
    if (exb == L_EX) STAMP(20);
    const u32 me = exb + wave * 3072 + lane * 16, ot = exb + (wave ^ 1) * 3072 + lane * 16;
    ldsw128(me, b0); ldsw128(me + 1024, b1); ldsw128(me + 2048, b2);
    extra();
    v16f d1 = {}, d2 = {};
    d1 = mfma32(asbf(wpf[0][0]), asbf(b0), d1); d2 = mfma32(asbf(wpf[1][0]), asbf(b0), d2);
    d1 = mfma32(asbf(wpf[0][1]), asbf(b1), d1); d2 = mfma32(asbf(wpf[1][1]), asbf(b1), d2);
    d1 = mfma32(asbf(wpf[0][2]), asbf(b2), d1); d2 = mfma32(asbf(wpf[1][2]), asbf(b2), d2);
    bar();
    const v4u c0 = ldsr128(ot), c1 = ldsr128(ot + 1024), c2 = ldsr128(ot + 2048);
    d1 = mfma32(asbf(wpf[0][0]), asbf(c0), d1); d2 = mfma32(asbf(wpf[1][0]), asbf(c0), d2);
    d1 = mfma32(asbf(wpf[0][1]), asbf(c1), d1); d2 = mfma32(asbf(wpf[1][1]), asbf(c1), d2);
    d1 = mfma32(asbf(wpf[0][2]), asbf(c2), d1); d2 = mfma32(asbf(wpf[1][2]), asbf(c2), d2);
    float pv[20];
#pragma unroll
    for (int i = 0; i < 16; ++i) pv[i] = d1[i];
#pragma unroll
    for (int i = 0; i < 4; ++i) pv[16 + i] = d2[i];
    softmax20(pv, o);
  };
  {
    const int tok = 32 * TT + i32;
    const u32 rb = L_TB + hh * 24576 + tok * 192, fx = (tok >> 1) & 3;
    auto cad = [&](int cc) { return rb + ((cc ^ fx) << 4); };
    u32 pk[10];
    phi(aK, lk[0], lk[1], lk[2], lk[3], L_EX, pk, [&]() {
      ldsw128(cad(0) + KH * 128, v4u{pkbf(aV[0], aV[1]), pkbf(aV[2], aV[3]), pkbf(aV[4], aV[5]), pkbf(aV[6], aV[7])});
      ldsw128(cad(1) + KH * 128, v4u{pkbf(aV[8], aV[9]), pkbf(aV[10], aV[11]), pkbf(aV[12], aV[13]), pkbf(aV[14], aV[15])});
      ldsw128(cad(2) + KH * 128, v4u{pkbf(lv[0], lv[1]), pkbf(lv[2], lv[3]), KH ? 0u : 0x00003f80u, 0u});
    });
    ldsw128(cad(KH ? 7 : 4), v4u{pk[0], pk[1], pk[2], pk[3]});
    ldsw128(cad(KH ? 3 : 5), v4u{pk[4], pk[5], pk[6], pk[7]});
    ldsw64(cad(6) + KH * 8, v2u{pk[8], pk[9]});
  }
  WST(6);
  drain();
  bar();
  STAMP(5);
  const bool dog2 = b < 216;
  const int g2c = b % 54, g2k = b / 54;
  v4u gw[5];
  const int T4 = wave & 3, K2 = wave >> 2;
  const int tokE = 128 * P + 64 * TH + 16 * T4 + t16;
  v4u xr[3];
  if (wave < 4) {
    const int hd = wave >> 1, tl = wave & 1;
    u32 a0[2];
#pragma unroll
    for (int t = 0; t < 2; ++t) {
      const int tok = 8 * hh + 4 * t + q4;
      a0[t] = L_TB + hd * 24576 + tok * 192 + (((2 * g2 + (p4 >> 1)) ^ ((tok >> 1) & 3)) << 4) + 8 * (p4 & 1);
    }
    const u32 pa0 = a0[0] + (tl ? 0 : 64), pa1 = a0[1] + (tl ? 0 : 64);
    v16f d = {};
#pragma unroll
    for (int h2 = 0; h2 < 2; ++h2) {
      v8bf fv0[4], fv1[4], fp[4];
#pragma unroll
      for (int s = 0; s < 4; ++s) {
        const u32 o = (4 * h2 + s) * 3072;
        fp[s] = trfrag(pa0 + o, pa1 + o); fv0[s] = trfrag(a0[0] + o, a0[1] + o); fv1[s] = trfrag(a0[0] + o + 128, a0[1] + o + 128);
      }
#pragma unroll
      for (int s = 0; s < 4; ++s) { d = mfma32(fp[s], fv0[s], d); d = mfma32(fp[s], fv1[s], d); }
    }
    const u32 base = WS_KVP + (J * 16 + P) * KVP_STRIDE + hd * 3360 + i32 * 16 + hh * 336;
    if (i32 < 21) {
      if (tl == 0) {
#pragma unroll
        for (int rq = 0; rq < 3; ++rq) stb128c(rs_ws, base + rq * 672, __builtin_bit_cast(v4u, v4f{d[4 * rq], d[4 * rq + 1], d[4 * rq + 2], d[4 * rq + 3]}));
      }
      stb128c(rs_ws, base + (3 + tl) * 672, __builtin_bit_cast(v4u, v4f{d[12], d[13], d[14], d[15]}));
    }
    drain();
    if (lane == 0) __hip_atomic_store((GAS u32*)(ws + WS_FLAG + (0 * 16 + J) * 256 + (P * 4 + wave) * 4), tag, RLX_AGENT);
    STAMP(6);
  } else {
    {
      const int e = (lane & 31) < 20 ? (lane & 31) : 19;
      if (wave < 7) {
        const u32 kk = ((g2k < 4 ? g2k : 3) * 40 + 20 * hh + e) * 16;
        const u32 o1 = wave == 4 ? 3 * 2560 + kk : wave == 5 ? (hh ? 5 : 2) * 2560 + CT * 320 + e * 16 : 4 * 2560 + CT * 320 + e * 16;
        v4u gs, gc;
        gather2(rs_ws, WS_MODG + o1, WS_MODG + 4 * 2560 + kk, tag, gs, gc);
        ldsw128f(wave == 4 ? L_G2S + (20 * hh + e) * 16 : wave == 5 ? L_WPHI + (20 * hh + e) * 16 : L_WPHI + 640 + e * 16, unp4(gs, wave == 6 ? 1.f : 0.f));
        if (wave == 4) ldsw128f(L_G2A + (20 * hh + e) * 16, unp4(gc, 1.f));
      }
    }
    if (dog2) {
      const rsrc_t rs_gu = mkrs(g2c < 27 ? wg : wu, DM * FF * 2);
      const u32 vo = (160 * g2k + 40 * (wave - 4) + (lane >> 3)) * 3456 + (g2c < 27 ? g2c : g2c - 27) * 128 + (lane & 7) * 16;
#pragma unroll
      for (int i = 0; i < 5; ++i) gw[i] = ldb128(rs_gu, vo, i * 8 * 3456);
    }
#pragma unroll
    for (int i = 0; i < 3; ++i) { const int it = i * 64 + lane < 160 ? i * 64 + lane : 159, tk = it / 10; xr[i] = ldb128(rs_x, (128 * P + 64 * TH + 16 * T4 + tk) * 1280 + (it - tk * 10) * 16, CT * 160); }
  }
  SB();
  WST(7);
  v4u rg[3], rd[2];
  {
    const rsrc_t rr = mkrs((wave >> 2) ? wu : wg, DM * FF * 2), rs_d = mkrs(wd, FF * DM * 2);
    const int row = lane / 14; int ch = lane - row * 14; if (ch >= 2 * nt) ch -= 2;
    const u32 vg = lane < 56 ? row * 3456 + ch * 16 : OOB, vd = lane < 40 ? l10 * 1280 + c10 * 16 : OOB;
    sfor<0, 3>([&](auto I) { const int gq = (wave & 3) + 4 * I.v; rg[I.v] = ldb128(rr, gq < 10 ? vg : OOB, t0 * 32 + (40 * P + 4 * gq) * 3456); });
    sfor<0, 2>([&](auto I) { rd[I.v] = ldb128(rs_d, vd, CT * 160 + kgrp(16 * rq27 + 2 * wave + I.v) * 5120); });
  }
  SB();
  v4u pv[16];
  {
    v16f aQ = {}, aQL = {};
    const u32 bq0 = opq(L_RQ + bmain + KH * 5120), bq1 = opq(L_RQ + rowb80 + KH * 5120 + ((p4 & 1) ? 72 : 0));
    constexpr auto qoff = [](int st) { return (u32)((st >> 2) * 10240 + (st & 3) * 1280); };
    v8bf wf[6];
#pragma unroll
    for (int f = 0; f < 6; ++f) { const u32 bb = (f & 1) ? bq1 : bq0; wf[f] = trfrag(bb + qoff(f >> 1), bb + qoff(f >> 1) + 80); }
    SB();
#pragma unroll
    for (int n = 0; n < 40; ++n) {
      const int st = n >> 1;
      const v8bf hb = asbf(xf[st & 1][st >> 1]);
      if (n & 1) aQL = mfma32(wf[n % 6], hb, aQL); else aQ = mfma32(wf[n % 6], hb, aQ);
      if (n + 6 < 40) { const u32 bb = (n & 1) ? bq1 : bq0; const u32 o = qoff((n + 6) >> 1); wf[n % 6] = trfrag(bb + o, bb + o + 80); }
      SB();
    }
    asm volatile("" : "+v"(aQ), "+v"(aQL));
    WST(8);
    sfor<0, 3>([&](auto I) { const int gq = (wave & 3) + 4 * I.v, row = 40 * P + 4 * gq;
      astorec(rg[I.v], rs_ws, (lane < 56 && gq < 10) ? vlin : OOB, WS_GU + J * 286720 + (row >> 6) * 28672 + (wave >> 2) * 14336 + (row & 63) * 224); });
    sfor<0, 2>([&](auto I) { astorec(rd[I.v], rs_ws, (lane < 40 && rq27 < 27) ? vlin : OOB, WS_WD + CT * 276480 + (16 * rq27 + 2 * wave + I.v) * 640); });
    WST(9);
    if (wave == 0) poll_line(ws, WS_FLAG + (0 * 16 + J) * 256 + lane * 4, tag);
    WST(10);
    STAMP(7);
    u32 pq[10];
    phi(aQ, aQL[0], aQL[1], aQL[2], aQL[3], L_XS, pq, [&]() {
      const u32 c = (tid < 420 ? tid : 419) * 16;
      sfor<0, 16>([&](auto I) { aloadc<0>(pv[I.v], rs_ws, wave < 7 ? c : OOB, WS_KVP + (J * 16 + I.v) * KVP_STRIDE); });
      SB();
    });
    STAMP(8);
    const u32 pb = L_XS + 24576 + TT * 5120 + lane * 16;
    ldsw128(pb + (KH ? 3072 : 0), v4u{pq[0], pq[1], pq[2], pq[3]});
    ldsw128(pb + (KH ? 4096 : 1024), v4u{pq[4], pq[5], pq[6], pq[7]});
    ldsw64(pb + 2048 + KH * 8, v2u{pq[8], pq[9]});
  }
  WST(11);
  auto wo1 = [&](int e) { dma16c(rs_ws, vlin, WS_WO + J * 102400 + e * 1024, e * 1024); };
  {
    sfor<0, WO_EARLY>([&](auto I) { wo1(8 * I.v + wave); });
    await4<WO_EARLY + 12>(pv[0], pv[1], pv[2], pv[3]);
    v4f sum = (__builtin_bit_cast(v4f, pv[0]) + __builtin_bit_cast(v4f, pv[1])) + (__builtin_bit_cast(v4f, pv[2]) + __builtin_bit_cast(v4f, pv[3]));
    sfor<1, 4>([&](auto G) {
      constexpr int g = G.v;
      await4<WO_EARLY + 12 - 4 * g>(pv[4 * g], pv[4 * g + 1], pv[4 * g + 2], pv[4 * g + 3]);
      sum += (__builtin_bit_cast(v4f, pv[4 * g]) + __builtin_bit_cast(v4f, pv[4 * g + 1])) + (__builtin_bit_cast(v4f, pv[4 * g + 2]) + __builtin_bit_cast(v4f, pv[4 * g + 3]));
    });
    const int c = tid, hd = c >= 210, rem = c - hd * 210, fq = rem / 21, d = rem - fq * 21;
    if (tid < 420) ldsw64(L_KVB + hd * 1680 + d * 80 + fq * 8, v2u{pkbf(sum[0], sum[1]), pkbf(sum[2], sum[3])});
  }
  WST(12);
  bar();
  STAMP(9);
  {
    const bool act = ((i32 >> 2) & 1) == hh;
    const int rq = i32 >> 3;
    const int om = rq < 2 ? 8 * KH + 4 * rq + (i32 & 3) : 16 + (i32 & 3);
    const int sg = rq == 3 ? 20 : (hh ? (om + 4) % 20 : (om + 16) % 20);
    const u32 a0 = act ? L_KVB + hh * 1680 + sg * 80 : L_ZERO, st = act ? 16 : 0;
    v4u ka[5], pqf[5];
#pragma unroll
    for (int s = 0; s < 5; ++s) { ka[s] = ldsr128(a0 + s * st); pqf[s] = ldsr128(L_XS + 24576 + TT * 5120 + lane * 16 + s * 1024); }
    v16f d = {};
#pragma unroll
    for (int s = 0; s < 5; ++s) d = mfma32(asbf(ka[s]), asbf(pqf[s]), d);
    const float rd = 1.0f / (d[12] + 1e-6f);
    const int tok = 128 * P + 32 * TT + i32;
    const u32 ob = WS_O + ((tok >> 4) * 80 + 5 * J) * 256 + (tok & 15) * 16;
    stb128c(rs_ws, ob + hh * 768 + KH * 256, v4u{pkbf(d[0] * rd, d[1] * rd), pkbf(d[2] * rd, d[3] * rd), pkbf(d[4] * rd, d[5] * rd), pkbf(d[6] * rd, d[7] * rd)});
    if (KH == 0) stb64c(rs_ws, ob + 512 + hh * 8, v2u{pkbf(d[8] * rd, d[9] * rd), pkbf(d[10] * rd, d[11] * rd)});
  }
  publish(ws, WS_FLAG + (1 * 16 + P) * 256 + J * 4, tag);
  if (tid == 64) __hip_atomic_store((GAS u32*)(ws + WS_FLAG + (4 * 16 + CT) * 256 + rq27 * 4), tag, RLX_AGENT);
  STAMP(10);
  auto gu1 = [&](int stage, u32 slot, int pc) { dmamc<56>(rs_ws, vlin, WS_GU + J * 286720 + wrap(stage + R3, 10) * 28672 + pc * 896, slot + pc * 896 + (pc >> 4) * 128); };
  if (wave >= 1 && wave < 4) {
#pragma unroll 1
    for (int e = 8 * WO_EARLY + wave - 1; e < 100; e += 3) wo1(e);
    if (wave == 3) poll_line(ws, WS_FLAG + (4 * 16 + CT) * 256 + i32 * 4, tag);
    drain();
  }
  v2u xq[5];
  if (wave >= 4) {
    const u32 xb = 102400 + T4 * 2816;
#pragma unroll
    for (int i = 0; i < 3; ++i) { const int it = i * 64 + lane < 160 ? i * 64 + lane : 159, tk = it / 10; ldsw128(xb + tk * 176 + (it - tk * 10) * 16, xr[i]); }
#pragma unroll
    for (int t = 0; t < 5; ++t) { xq[t] = ldsr64(xb + t16 * 176 + t * 32 + g4 * 8); asm volatile("" : "+v"(xq[t])); }
  }
  SB();
  if (dog2 && wave >= 4) {
    float sa[8] = {}, sb2[8] = {};
#pragma unroll
    for (int i = 0; i < 5; ++i) {
      const float ca = ldsrf(L_G2A + (40 * (wave - 4) + 8 * i + (lane >> 3)) * 4), cs = ldsrf(L_G2S + (40 * (wave - 4) + 8 * i + (lane >> 3)) * 4);
#pragma unroll
      for (int e = 0; e < 4; ++e) {
        const float lo = bflo(gw[i][e]), hi = bfhi(gw[i][e]);
        sa[2 * e] += ca * lo; sa[2 * e + 1] += ca * hi; sb2[2 * e] += cs * lo; sb2[2 * e + 1] += cs * hi;
      }
    }
#pragma unroll
    for (int e = 0; e < 8; ++e) { sa[e] = xsum32(xsum16(dppadd<DPP_ROR8>(sa[e]))); sb2[e] = xsum32(xsum16(dppadd<DPP_ROR8>(sb2[e]))); }
    if (lane < 8) {
      const u32 a = L_G2R + (wave - 4) * 512 + lane * 32;
      ldsw128f(a, v4f{sa[0], sa[1], sa[2], sa[3]}); ldsw128f(a + 16, v4f{sa[4], sa[5], sa[6], sa[7]});
      ldsw128f(a + 256, v4f{sb2[0], sb2[1], sb2[2], sb2[3]}); ldsw128f(a + 272, v4f{sb2[4], sb2[5], sb2[6], sb2[7]});
    }
  }
  SB();
  WST(13);
  wait_line(ws, WS_FLAG + (1 * 16 + P) * 256, tag, wave, lane, 15);
  STAMP(11);
  const u32 b160 = (16 * (g4 >> 1) + 4 * (g4 & 1) + q4) * 160 + p4 * 8 + K2 * 5120;
  constexpr u32 L_P2X = 94720;
  float x1[20];
  {
    v4u of[10];
    const u32 ab = WS_O + ((8 * P + 4 * TH + T4) * 80 + 4 * K2) * 256 + lane * 16;
    sfor<0, 10>([&](auto S) { aloadc<(S.v & 1) * 2048>(of[S.v], rs_ws, ab, (S.v >> 1) * 4096); });
    sfor<0, 4>([&](auto I) { gu1(0, p3slot(0), 4 * wave + I.v); });
    const u32 bb0 = opq(b160), bb1 = opq(b160 + 51200);
    v8bf wf[5];
#pragma unroll
    for (int t = 0; t < 5; ++t) wf[t] = trfrag(bb0 + t * 32, bb0 + t * 32 + 1280);
    v4f acc[5] = {};
    SB();
    sfor<0, 10>([&](auto S) {
      constexpr int s = S.v;
      await1<13 - s>(of[s]);
#pragma unroll
      for (int t = 0; t < 5; ++t) {
        acc[t] = mfma16(wf[t], asbf(of[s]), acc[t]);
        if (s < 9) { const u32 bb = s + 1 >= 5 ? bb1 : bb0; const u32 o = ((s + 1) % 5) * 10240 + t * 32; wf[t] = trfrag(bb + o, bb + o + 1280); }
        SB();
      }
    });
    asm volatile("" : "+v"(acc[0]), "+v"(acc[1]), "+v"(acc[2]), "+v"(acc[3]), "+v"(acc[4]));
    WST(14);
    bar();
    const u32 xb = L_P2X + T4 * 5120 + lane * 16;
    if (K2 == 0) {
#pragma unroll
      for (int t = 0; t < 5; ++t) ldsw128f(xb + t * 1024, acc[t]);
    }
    bar();
    if (K2 == 1) {
      float sum = 0.f;
#pragma unroll
      for (int t = 0; t < 5; ++t) {
        const v4f a = acc[t] + ldsr128f(xb + t * 1024);
        const v4f ga = ldsr128f(L_WPHI + t * 64 + g4 * 16), A2 = ldsr128f(L_WPHI + 640 + t * 64 + g4 * 16);
        const u32 p0 = pkbf(bflo(xq[t][0]) + ga[0] * a[0], bfhi(xq[t][0]) + ga[1] * a[1]);
        const u32 p1 = pkbf(bflo(xq[t][1]) + ga[2] * a[2], bfhi(xq[t][1]) + ga[3] * a[3]);
        x1[4 * t] = bflo(p0); x1[4 * t + 1] = bfhi(p0); x1[4 * t + 2] = bflo(p1); x1[4 * t + 3] = bfhi(p1);
        sum += (x1[4 * t] + x1[4 * t + 1]) + (x1[4 * t + 2] + x1[4 * t + 3]);
        const v2u uu = {pkbf(x1[4 * t] * A2[0], x1[4 * t + 1] * A2[1]), pkbf(x1[4 * t + 2] * A2[2], x1[4 * t + 3] * A2[3])};
        stb64c(rs_ws, WS_U + ((tokE >> 4) * 80 + CT * 10 + 2 * t + (g4 >> 1)) * 256 + t16 * 16 + (g4 & 1) * 8, uu);
      }
      const float mt = xsum32(xsum16(sum)) * (1.f / 80.f);
      float m2 = 0.f;
#pragma unroll
      for (int i = 0; i < 20; ++i) { const float dd = x1[i] - mt; m2 += dd * dd; }
      m2 = xsum32(xsum16(m2));
      if (g4 == 0) stb64c(rs_ws, WS_STAT + ((P * 8 + CT) * 128 + (tokE & 127)) * 8, v2u{__float_as_uint(mt), __float_as_uint(m2)});
      drain();
      if (lane == 0) __hip_atomic_store((GAS u32*)(ws + WS_FLAG + (2 * 16 + P) * 256 + (J * 4 + T4) * 4), tag, RLX_AGENT);
    } else {
      if (dog2 && wave == 0) {
        const u32 a = L_G2R + lane * 4;
        const float av = (ldsrf(a) + ldsrf(a + 512)) + (ldsrf(a + 1024) + ldsrf(a + 1536));
        const float bv = (ldsrf(a + 256) + ldsrf(a + 768)) + (ldsrf(a + 1280) + ldsrf(a + 1792));
        stb64c(rs_ws, WS_AB + (g2k * 3456 + g2c * 64 + lane) * 16, v2u{__float_as_uint(av), tag});
        stb64c(rs_ws, WS_AB + (g2k * 3456 + g2c * 64 + lane) * 16 + 8, v2u{__float_as_uint(bv), tag});
      }
#pragma unroll
      for (int i = 0; i < 20; ++i) x1[i] = 0.f;
    }
  }
  STAMP(12);
  WST(15);
  auto wd1 = [&](int e) { dma16c(rs_ws, vlin, WS_WD + CT * 276480 + wrap(e + 10 * R4, 270) * 1024, wrap(wrap(e + 120, 150), 150) * 1024); };
  {
    if (wave > 0 && wave < 4) {
#pragma unroll 1
      for (int pc = wave - 1; pc < 32; pc += 3) { gu1(1, 0, pc); waitv<3>(); }
    }
    drain();
    WST(16);
    wait_line(ws, WS_FLAG + (2 * 16 + P) * 256, tag, wave, lane, 63);
    STAMP(13);
    constexpr int U0 = 8;
    v4u uf[20];
    const u32 ab = WS_U + ((8 * P + 2 * TT + g2) * 80 + 2 * KH + hh) * 256 + t16 * 16;
    sfor<0, U0>([&](auto N) { aloadc<(N.v & 1) * 1024>(uf[N.v], rs_ws, ab, wrap((N.v >> 1) + R3, 10) * 2048); });
    SB();
    const u32 bw = g2 * P3UP + (16 * KH + 8 * hh + q4) * 224 + 16 * (p4 & 1);
    const u32 tm = (KH ? nt - 3 : 0) * 32, to = (KH ? 0 : nt - 3) * 32;
    u32 bsl[3][3];
#pragma unroll
    for (int z = 0; z < 3; ++z) {
      bsl[0][z] = opq(bw + 8 * (p4 >> 1) + tm + z * 2 * P3SLOT);
      bsl[1][z] = opq(bw + 8 * ((p4 >> 1) ^ KH) + 96 + z * 2 * P3SLOT);
      bsl[2][z] = opq(bw + 8 * (p4 >> 1) + to + z * 2 * P3SLOT);
    }
    auto wad = [&](int n, int l) {
      const int sl = ((n >> 1) + 4) % 5, gi = l < 3 ? 0 : l == 3 ? 1 : 2;
      return bsl[gi][sl >> 1] + (sl & 1) * P3SLOT + (n & 1) * 7168 + (l < 3 ? l : l == 3 ? 0 : l - 4) * 32;
    };
    v8bf wf[7];
#pragma unroll
    for (int l = 0; l < 7; ++l) wf[l] = trfrag(wad(0, l), wad(0, l) + 896);
    v16f acc[7] = {};
    v4u st[2], abg[4];
    const u32 stoff = WS_STAT + ((P * 8 + 4 * hh + 2 * (lane & 1)) * 128 + 32 * TT + (i32 & ~1)) * 8;
    const int abe = (wave & 3) * 56 + (lane < 56 ? lane : 55);
    u32 aboff;
    { const int mat = abe >= 112, c = abe - 112 * mat; aboff = WS_AB + (mat * 1728 + (t0 * 16 + c < 1728 ? t0 * 16 + c : 1727)) * 16; }
    constexpr auto nops = [](int m) { return (m < 18 ? 1 : 0) + (m < 4 ? 4 : m < 12 ? 2 : m < 16 ? 2 - (m & 1) : m < 18 ? 1 : 0); };
    constexpr auto after = [=](int n) { int c = n < U0 ? U0 - 1 - n : nops(n - U0) - 1; for (int m = n < U0 ? 0 : n - U0 + 1; m < n; ++m) c += nops(m); return c; };
    SB();
    sfor<0, 20>([&](auto N) {
      constexpr int n = N.v, stage = n >> 1;
      if constexpr ((n & 1) == 0 && stage >= 1) {
        if constexpr (stage <= 4) { constexpr auto sum = [=](int a, int b2) { int c = 0; for (int m = a; m < b2; ++m) c += nops(m); return c; }; waitv<sum(stage, n)>(); }
        else if constexpr (stage <= 8) waitv<nops(n - 4) + nops(n - 3) + nops(n - 2) + nops(n - 1)>();
        rawbar();
        SB();
      }
      await1<after(n)>(uf[n]);
      sfor<0, 7>([&](auto L) {
        constexpr int l = L.v;
        acc[l] = mfma32(wf[l], asbf(uf[n]), acc[l]);
        if constexpr (n < 19) wf[l] = trfrag(wad(n + 1, l), wad(n + 1, l) + 896);
        if constexpr (l == 0) {
          if constexpr (n + U0 < 20) aloadc<((n + U0) & 1) * 1024>(uf[n + U0], rs_ws, ab, wrap(((n + U0) >> 1) + R3, 10) * 2048);
          else if constexpr (n + U0 < 22) aloadc<0>(st[n + U0 - 20], rs_ws, stoff, (n + U0 - 20) * 1024);
          else if constexpr (n + U0 < 26) aloadc<0>(abg[n + U0 - 22], rs_ws, aboff, (n + U0 - 22) * 3456 * 16);
        }
        if constexpr (n < 4 && (l == 1 || l == 2 || l == 4 || l == 5)) gu1(n + 2, p3slot(n + 2), 4 * wave + (l < 3 ? l - 1 : l - 2));
        if constexpr (n >= 4 && (l == 2 || l == 5)) {
          constexpr int i = 2 * (n & 1) + (l == 5);
          if constexpr (stage <= 5) gu1(stage + 4, p3slot(stage + 4), 4 * wave + i);
          else if constexpr (stage <= 7) { if constexpr (i < 3) wd1(8 * (3 * (stage - 6) + i) + wave); }
          else if constexpr (stage == 8) { if constexpr (l == 2) wd1(8 * (6 + (n & 1)) + wave); }
        }
        SB();
      });
    });
    asm volatile("" : "+v"(acc[0]), "+v"(acc[1]), "+v"(acc[2]), "+v"(acc[3]), "+v"(acc[4]), "+v"(acc[5]), "+v"(acc[6]));
    STAMP(14);
    await4<1>(abg[0], abg[1], abg[2], abg[3]);
    asm volatile("" : "+v"(st[0]), "+v"(st[1]));
    if (wave < 4) {
      if (!__all(abg[0][1] == tag && abg[0][3] == tag && abg[1][1] == tag && abg[1][3] == tag && abg[2][1] == tag && abg[2][3] == tag && abg[3][1] == tag && abg[3][3] == tag))
        gather4(rs_ws, aboff, 3456 * 16, tag, abg[0], abg[1], abg[2], abg[3]);
      ldswf(L_AB + abe * 4, (__uint_as_float(abg[0][0]) + __uint_as_float(abg[1][0])) + (__uint_as_float(abg[2][0]) + __uint_as_float(abg[3][0])));
      ldswf(L_AB + 896 + abe * 4, (__uint_as_float(abg[0][2]) + __uint_as_float(abg[1][2])) + (__uint_as_float(abg[2][2]) + __uint_as_float(abg[3][2])));
    }
    float rs2, mr2;
    {
      constexpr int QP = 0xb1;
      const bool od = lane & 1;
      float mk[4], qk[4];
#pragma unroll
      for (int z = 0; z < 2; ++z) {
        const u32 km = od ? st[z][2] : st[z][0], kq = od ? st[z][3] : st[z][1], sm = od ? st[z][0] : st[z][2], sq = od ? st[z][1] : st[z][3];
        mk[z] = __uint_as_float(km); qk[z] = __uint_as_float(kq);
        mk[2 + z] = __uint_as_float(__builtin_amdgcn_update_dpp(0u, sm, QP, 0xf, 0xf, true));
        qk[2 + z] = __uint_as_float(__builtin_amdgcn_update_dpp(0u, sq, QP, 0xf, 0xf, true));
      }
      const float m = xsum32((mk[0] + mk[1]) + (mk[2] + mk[3])) * 0.125f;
      float q = 0.f;
#pragma unroll
      for (int z = 0; z < 4; ++z) { const float dm = mk[z] - m; q += qk[z] + 80.f * dm * dm; }
      rs2 = __builtin_amdgcn_rsqf(xsum32(q) * (1.f / DM) + 1e-5f); mr2 = -m * rs2;
    }
    const int tok = 128 * P + 32 * TT + i32;
    const u32 ob = WS_ACT + ((tok >> 4) * 216 + 2 * t0 + hh) * 256 + (tok & 15) * 16;
    const u32 me = opq(L_P3X + wave * 8192 + lane * 16), ot = opq(L_P3X + (wave ^ 1) * 8192 + lane * 16);
    auto snd = [&](const v16f& a, int rq, u32 o) { ldsw128f(me + o, v4f{a[4 * rq], a[4 * rq + 1], a[4 * rq + 2], a[4 * rq + 3]}); };
    auto rcv = [&](v16f& a, int rq, u32 o) { const v4f v = ldsr128f(ot + o); a[4 * rq] += v[0]; a[4 * rq + 1] += v[1]; a[4 * rq + 2] += v[2]; a[4 * rq + 3] += v[3]; };
    auto geglu4 = [&](const v16f& a, int r4, u32 cb, float (&o)[4]) {
      const v4f ag = ldsr128f(cb), au = ldsr128f(cb + 448), bg = ldsr128f(cb + 896), bu = ldsr128f(cb + 1344);
#pragma unroll
      for (int e = 0; e < 4; ++e) {
        const float gv = __builtin_fmaf(a[4 * r4 + e], rs2, __builtin_fmaf(ag[e], mr2, bg[e]));
        const float uv = __builtin_fmaf(a[8 + 4 * r4 + e], rs2, __builtin_fmaf(au[e], mr2, bu[e]));
        const float z = gv * __builtin_fmaf(gv * gv, -0.044715f * 2.302208198f, -2.302208198f);
        o[e] = gv * uv * __builtin_amdgcn_rcpf(1.f + __builtin_amdgcn_exp2f(z));
      }
    };
    const u32 abm = opq(L_AB + hh * 32 + tm * 2);
    auto fin = [&](int l) {
      float a0[4], a1[4];
      geglu4(acc[l], 0, abm + l * 64, a0); geglu4(acc[l], 1, abm + l * 64 + 16, a1);
      stb128c(rs_ws, ob + tm * 16 + l * 512, v4u{pkbf(a0[0], a0[1]), pkbf(a0[2], a0[3]), pkbf(a1[0], a1[1]), pkbf(a1[2], a1[3])});
    };
    bar();
    STAMP(21);
#pragma unroll
    for (int rq = 0; rq < 4; ++rq) { snd(acc[4], rq, rq * 1024); snd(acc[5], rq, 4096 + rq * 1024); }
    bar();
#pragma unroll
    for (int rq = 0; rq < 4; ++rq) { rcv(acc[0], rq, rq * 1024); rcv(acc[1], rq, 4096 + rq * 1024); }
    bar();
#pragma unroll
    for (int rq = 0; rq < 4; ++rq) snd(acc[6], rq, rq * 1024);
    snd(acc[3], 1, 4096); snd(acc[3], 3, 5120);
    SB();
    fin(0);
    bar();
#pragma unroll
    for (int rq = 0; rq < 4; ++rq) rcv(acc[2], rq, rq * 1024);
    rcv(acc[3], 0, 4096); rcv(acc[3], 2, 5120);
    fin(1); fin(2);
    if (nt == 7) {
      float a0[4];
      geglu4(acc[3], 0, L_AB + hh * 32 + 192 + KH * 16, a0);
      stb64c(rs_ws, ob + 3 * 512 + KH * 8, v2u{pkbf(a0[0], a0[1]), pkbf(a0[2], a0[3])});
    }
  }
  STAMP(22);
  WST(17);
  publish(ws, WS_FLAG + (3 * 16 + P) * 256 + J * 4, tag);
  STAMP(15);
  {
    if (wave != 0) {
#pragma unroll 1
      for (int e = 63 + wave; e < 150; e += 7) wd1(e);
    }
    WST(18);
    wait_line(ws, WS_FLAG + (3 * 16 + P) * 256, tag, wave, lane, 15);
    STAMP(16);
    constexpr int A0 = 8;
    v4u af[27];
    const u32 ab = WS_ACT + ((8 * P + 4 * TH + T4) * 216 + 4 * K2) * 256 + lane * 16;
    sfor<0, A0>([&](auto N) { aloadc<0>(af[N.v], rs_ws, ab, wrap(N.v + R4, 27) * 2048); });
    SB();
    u32 bs[3];
    bs[0] = opq(b160); bs[1] = opq(b160 + 51200); bs[2] = opq(b160 + 102400);
    auto wad = [&](int n) { const int lu = (n + 12) % 15; return bs[lu / 5] + (lu % 5) * 10240; };
    v8bf wf[5];
#pragma unroll
    for (int t = 0; t < 5; ++t) wf[t] = trfrag(wad(0) + t * 32, wad(0) + t * 32 + 1280);
    v4f acc[5] = {};
    constexpr auto nops = [](int m) { return (m + A0 < 27 ? 1 : 0) + ((m >= 4 && m < 16) ? ((m & 3) == 0 ? 2 : 1) : 0); };
    constexpr auto after = [=](int n) { int c = n < A0 ? A0 - 1 - n : nops(n - A0) - 1; for (int m = n < A0 ? 0 : n - A0 + 1; m < n; ++m) c += nops(m); return c; };
    SB();
    sfor<0, 27>([&](auto N) {
      constexpr int n = N.v;
      if constexpr (n == 4 || n == 8) { rawbar(); SB(); }
      if constexpr (n == 12 || n == 16 || n == 20) { waitv<nops(n - 4) + nops(n - 3) + nops(n - 2) + nops(n - 1)>(); rawbar(); SB(); }
      await1<after(n)>(af[n]);
      sfor<0, 5>([&](auto T) {
        constexpr int t = T.v;
        acc[t] = mfma16(wf[t], asbf(af[n]), acc[t]);
        if constexpr (n < 26) wf[t] = trfrag(wad(n + 1) + t * 32, wad(n + 1) + t * 32 + 1280);
        if constexpr (n + A0 < 27 && t == 0) aloadc<0>(af[n + A0], rs_ws, ab, wrap(n + A0 + R4, 27) * 2048);
        if constexpr (n >= 4 && n < 16 && (t == 2 || (t == 4 && (n & 3) == 0))) wd1(150 + 40 * ((n >> 2) - 1) + 8 * ((n & 3) == 0 ? (t == 4) : (n & 3) + 1) + wave);
        SB();
      });
    });
    asm volatile("" : "+v"(acc[0]), "+v"(acc[1]), "+v"(acc[2]), "+v"(acc[3]), "+v"(acc[4]));
    STAMP(17);
    const u32 xb = 92160 + T4 * 5120 + lane * 16;
    if (K2 == 0) {
#pragma unroll
      for (int t = 0; t < 5; ++t) ldsw128f(xb + t * 1024, acc[t]);
    }
    bar();
    if (K2 == 1) {
      const u32 ob = 112640 + T4 * 2816;
#pragma unroll
      for (int t = 0; t < 5; ++t) {
        const v4f a = acc[t] + ldsr128f(xb + t * 1024);
        const v4f gb = ldsr128f(L_WPHI + 320 + t * 64 + g4 * 16);
        ldsw64(ob + t16 * 176 + t * 32 + g4 * 8, v2u{pkbf(x1[4 * t] + gb[0] * a[0], x1[4 * t + 1] + gb[1] * a[1]), pkbf(x1[4 * t + 2] + gb[2] * a[2], x1[4 * t + 3] + gb[3] * a[3])});
      }
#pragma unroll
      for (int i = 0; i < 3; ++i) {
        const int it = i * 64 + lane, tk = it / 10, ch = it - tk * 10;
        if (it < 160) *(GAS v4u*)((GAS char*)out + (128 * P + 64 * TH + 16 * T4 + tk) * 1280 + CT * 160 + ch * 16) = ldsr128(ob + tk * 176 + ch * 16);
      }
    }
  }
  if (b == 0 && tid == 0) __hip_atomic_store((GAS u32*)(ws + WS_EPOCH), tag, RLX_AGENT);
  WST(19);
  STAMP(18);
}
}

extern "C" void kernel_launch(void* const* d_in, const int* in_sizes, int n_in, void* d_out, int out_size, void* d_ws, size_t ws_size, hipStream_t stream) {
  (void)in_sizes; (void)n_in; (void)out_size; (void)ws_size;
  (void)hipFuncSetAttribute((const void*)fused_kernel, hipFuncAttributeMaxDynamicSharedMemorySize, L_TOTAL);
  fused_kernel<<<256, 512, L_TOTAL, stream>>>((const u16*)d_in[0], (const u16*)d_in[1], (const u16*)d_in[2], (const u16*)d_in[3], (const u16*)d_in[5],
                                              (const u16*)d_in[6], (const u16*)d_in[7], (const u16*)d_in[8], (const u16*)d_in[9], (u16*)d_out, (char*)d_ws);
}
```

```cpp
#include <hip/hip_runtime.h>
#include <stdint.h>

typedef unsigned short u16;
typedef unsigned u32;
typedef unsigned long long u64;
typedef u32 v4u __attribute__((ext_vector_type(4)));
typedef u32 v2u __attribute__((ext_vector_type(2)));
typedef float v4f __attribute__((ext_vector_type(4)));
typedef float v2f __attribute__((ext_vector_type(2)));
typedef float v16f __attribute__((ext_vector_type(16)));
typedef short v4s __attribute__((ext_vector_type(4)));
typedef __bf16 v8bf __attribute__((ext_vector_type(8)));
typedef __bf16 v2bf __attribute__((ext_vector_type(2)));
typedef _Float16 v2h __attribute__((ext_vector_type(2)));
#define LAS __attribute__((address_space(3)))
#define GAS __attribute__((address_space(1)))
#define DI __device__ __forceinline__
#define RLX_AGENT __ATOMIC_RELAXED, __HIP_MEMORY_SCOPE_AGENT
#define SB() __builtin_amdgcn_sched_barrier(0)
typedef __amdgpu_buffer_rsrc_t rsrc_t;

#define ROT_ON 1
namespace {
constexpr int SEQ = 2048, DM = 640, FF = 1728;
constexpr u32 WS_EPOCH = 0;
constexpr u32 WS_FLAG = 1024;
constexpr u32 WS_ST1 = 61440;
constexpr u32 WS_MODG = WS_ST1 + 32768;
constexpr u32 WS_AB = WS_MODG + 4 * 30720;
constexpr u32 WS_KVP = WS_AB + 4 * 3456 * 16;
constexpr u32 KVP_STRIDE = 6784;
constexpr u32 WS_STAT = WS_KVP + 256 * KVP_STRIDE;
constexpr u32 WS_O = WS_STAT + SEQ * 64;
constexpr u32 WS_U = WS_O + SEQ * DM * 2;
constexpr u32 WS_ACT = WS_U + SEQ * DM * 2;
constexpr u32 WS_GU = WS_ACT + SEQ * FF * 2;
constexpr u32 WS_WD = WS_GU + 16 * 286720;
constexpr u32 WS_WQ = WS_WD + 8 * 276480;
constexpr u32 WS_WO = WS_WQ + 16 * 153600;
constexpr u32 WS_T = WS_WO + 16 * 102400;
constexpr u32 WS_END = WS_T + SEQ * DM * 2;
constexpr u32 WS_DBG = (WS_END + 4095) & ~4095u;
static_assert(WS_KVP % 128 == 0 && WS_STAT % 128 == 0 && WS_O % 256 == 0, "align");
constexpr u32 L_RQ = 102400;
constexpr u32 L_RAW = 102400;
constexpr u32 L_MISC = 153600;
constexpr u32 L_COA = L_MISC, L_COS = L_MISC + 2560;
constexpr u32 L_KVB = L_MISC + 5120;
constexpr u32 L_ZERO = L_KVB + 3360;
constexpr u32 L_WPHI = L_ZERO + 16;
constexpr u32 L_SYNC = L_WPHI + 1600;
constexpr u32 L_TOTAL = 163840;
constexpr u32 L_G2A = L_COA, L_G2S = L_COA + 640;
constexpr u32 L_G2R = L_COA + 1280;
constexpr u32 L_AB = L_COA + 3328;
static_assert(L_SYNC + 32 <= L_TOTAL && L_AB + 1792 <= L_KVB, "lds");
constexpr int WO_EARLY = 4, KV_THR = 8, GU_THR = 6;
#define CODE_PF 49152
#define CODE_BACK 0
constexpr u32 SPIN_CAP = 1u << 15;
constexpr int AUX_SC1 = 16;
constexpr u32 OOB = 0x7ffffff0u;

DI float bflo(u32 v) { return __uint_as_float(v << 16); }
DI float bfhi(u32 v) { return __uint_as_float(v & 0xffff0000u); }
DI u32 pkbf(float a, float b) { v2f t = {a, b}; return __builtin_bit_cast(u32, __builtin_convertvector(t, v2bf)); }
DI float rbf(float a) { return bflo(pkbf(a, a)); }
DI void bar() { asm volatile("s_waitcnt lgkmcnt(0)\n\ts_barrier" ::: "memory"); }
DI void rawbar() { asm volatile("s_barrier" ::: "memory"); }
DI void drain() { asm volatile("s_waitcnt vmcnt(0)" ::: "memory"); }
template <int N> DI void waitv() { asm volatile("s_waitcnt vmcnt(%0)" ::"i"(N) : "memory"); }
template <int I> struct IC { static constexpr int v = I; };
template <int B, int E, class F> DI void sfor(F&& f) { if constexpr (B < E) { f(IC<B>{}); sfor<B + 1, E>(f); } }
DI rsrc_t mkrs(const void* p, u32 bytes) { return __builtin_amdgcn_make_buffer_rsrc((void*)p, 0, bytes, 0x00020000); }
DI void dma16(rsrc_t rs, u32 voff, u32 soff, u32 lds) {
  asm volatile("s_mov_b32 m0, %0\n\ts_nop 0\n\tbuffer_load_dwordx4 %1, %2, %3 offen lds" ::"s"(lds), "v"(voff), "s"(rs), "s"(soff) : "memory");
}
template <int NL> DI void dmam(rsrc_t rs, u32 voff, u32 soff, u32 lds) {
  asm volatile("s_mov_b32 m0, %0\n\ts_mov_b32 exec_hi, %4\n\tbuffer_load_dwordx4 %1, %2, %3 offen lds\n\ts_mov_b32 exec_hi, -1"
               ::"s"(lds), "v"(voff), "s"(rs), "s"(soff), "i"((1u << (NL - 32)) - 1u) : "memory");
}
DI void dma16c(rsrc_t rs, u32 voff, u32 soff, u32 lds) {
  asm volatile("s_mov_b32 m0, %0\n\ts_nop 0\n\tbuffer_load_dwordx4 %1, %2, %3 offen sc1 lds" ::"s"(lds), "v"(voff), "s"(rs), "s"(soff) : "memory");
}
template <int NL> DI void dmamc(rsrc_t rs, u32 voff, u32 soff, u32 lds) {
  asm volatile("s_mov_b32 m0, %0\n\ts_mov_b32 exec_hi, %4\n\tbuffer_load_dwordx4 %1, %2, %3 offen sc1 lds\n\ts_mov_b32 exec_hi, -1"
               ::"s"(lds), "v"(voff), "s"(rs), "s"(soff), "i"((1u << (NL - 32)) - 1u) : "memory");
}
DI u32 opq(u32 v) { asm volatile("" : "+v"(v)); return v; }
DI v4s trd(u32 addr) { return __builtin_amdgcn_ds_read_tr16_b64_v4i16((LAS v4s*)(size_t)addr); }
DI v8bf trfrag(u32 a0, u32 a1) {
  v4s a = trd(a0), b = trd(a1);
  return __builtin_bit_cast(v8bf, __builtin_shufflevector(a, b, 0, 1, 2, 3, 4, 5, 6, 7));
}
DI v16f mfma32(v8bf a, v8bf b, v16f c) { return __builtin_amdgcn_mfma_f32_32x32x16_bf16(a, b, c, 0, 0, 0); }
DI v4f mfma16(v8bf a, v8bf b, v4f c) { return __builtin_amdgcn_mfma_f32_16x16x32_bf16(a, b, c, 0, 0, 0); }
DI v8bf asbf(v4u v) { return __builtin_bit_cast(v8bf, v); }
DI v4u ldsr128(u32 a) { return *(LAS v4u*)(size_t)a; }
DI v4f ldsr128f(u32 a) { return *(LAS v4f*)(size_t)a; }
DI v2u ldsr64(u32 a) { return *(LAS v2u*)(size_t)a; }
DI void ldsw128(u32 a, v4u v) { *(LAS v4u*)(size_t)a = v; }
DI void ldsw128f(u32 a, v4f v) { *(LAS v4f*)(size_t)a = v; }
DI void ldsw64(u32 a, v2u v) { *(LAS v2u*)(size_t)a = v; }
DI float ldsrf(u32 a) { return *(LAS float*)(size_t)a; }
DI void ldswf(u32 a, float v) { *(LAS float*)(size_t)a = v; }
DI float mixlo(u32 t, float a, float s) { float r; asm("v_fma_mix_f32 %0, %1, %2, %3 op_sel_hi:[1,0,0]" : "=v"(r) : "v"(t), "v"(a), "v"(s)); return r; }
DI float mixhi(u32 t, float a, float s) { float r; asm("v_fma_mix_f32 %0, %1, %2, %3 op_sel:[1,0,0] op_sel_hi:[1,0,0]" : "=v"(r) : "v"(t), "v"(a), "v"(s)); return r; }
DI float xsum16(float v) {
  auto r = __builtin_amdgcn_permlane16_swap(__float_as_uint(v), __float_as_uint(v), false, false);
  return __uint_as_float(r[0]) + __uint_as_float(r[1]);
}
DI float xsum32(float v) {
  auto r = __builtin_amdgcn_permlane32_swap(__float_as_uint(v), __float_as_uint(v), false, false);
  return __uint_as_float(r[0]) + __uint_as_float(r[1]);
}
template <int CTRL> DI float dppadd(float v) {
  return v + __uint_as_float(__builtin_amdgcn_update_dpp(0u, __float_as_uint(v), CTRL, 0xf, 0xf, true));
}
constexpr int DPP_ROR4 = 0x124, DPP_ROR8 = 0x128;
DI float red8(const float (&a)[8], int lane) {
  float b4[4], c[2];
#pragma unroll
  for (int e = 0; e < 4; ++e) { auto r = __builtin_amdgcn_permlane32_swap(__float_as_uint(a[e]), __float_as_uint(a[4 + e]), false, false); b4[e] = __uint_as_float(r[0]) + __uint_as_float(r[1]); }
#pragma unroll
  for (int e = 0; e < 2; ++e) { auto r = __builtin_amdgcn_permlane16_swap(__float_as_uint(b4[e]), __float_as_uint(b4[2 + e]), false, false); c[e] = __uint_as_float(r[0]) + __uint_as_float(r[1]); }
  const float x = dppadd<DPP_ROR8>(c[0]), y = dppadd<DPP_ROR8>(c[1]);
  return (lane & 8) ? y : x;
}
DI v4u ldb128(rsrc_t rs, u32 vo, u32 so) { return __builtin_amdgcn_raw_buffer_load_b128(rs, vo, so, 0); }
DI v4u ldb128c(rsrc_t rs, u32 vo, u32 so) { return __builtin_amdgcn_raw_buffer_load_b128(rs, vo, so, AUX_SC1); }
template <int OFF> DI void aload(v4u& r, rsrc_t rs, u32 vo, u32 so) {
  asm volatile("buffer_load_dwordx4 %0, %1, %2, %3 offen offset:%4" : "=v"(r) : "v"(vo), "s"(rs), "s"(so), "i"(OFF) : "memory");
}
template <int OFF> DI void aloadc(v4u& r, rsrc_t rs, u32 vo, u32 so) {
  asm volatile("buffer_load_dwordx4 %0, %1, %2, %3 offen offset:%4 sc1" : "=v"(r) : "v"(vo), "s"(rs), "s"(so), "i"(OFF) : "memory");
}
template <int N> DI void await1(v4u& a) { asm volatile("s_waitcnt vmcnt(%1)" : "+v"(a) : "i"(N) : "memory"); }
template <int N> DI void await2(v4u& a, v4u& b) { asm volatile("s_waitcnt vmcnt(%2)" : "+v"(a), "+v"(b) : "i"(N) : "memory"); }
template <int N> DI void await4(v4u& a, v4u& b, v4u& c, v4u& d) { asm volatile("s_waitcnt vmcnt(%4)" : "+v"(a), "+v"(b), "+v"(c), "+v"(d) : "i"(N) : "memory"); }
DI void astorec(v4u v, rsrc_t rs, u32 vo, u32 so) { u32 o = vo + so; asm volatile("" : "+v"(o)); __builtin_amdgcn_raw_buffer_store_b128(v, rs, o, 0, AUX_SC1); }
DI int sopq(int v) { asm volatile("" : "+s"(v)); return v; }
template <int N> DI void await3(v4u& a, v4u& b, v4u& c) { asm volatile("s_waitcnt vmcnt(%3)" : "+v"(a), "+v"(b), "+v"(c) : "i"(N) : "memory"); }
DI void stb128c(rsrc_t rs, u32 vo, v4u v) { __builtin_amdgcn_raw_buffer_store_b128(v, rs, vo, 0, AUX_SC1); }
DI void stb64c(rsrc_t rs, u32 vo, v2u v) { __builtin_amdgcn_raw_buffer_store_b64(v, rs, vo, 0, AUX_SC1); }

#define WST(i) do {} while (0)
#define STAMP(i) do {} while (0)

DI void setflag(char* ws, u32 off, u32 tag) { (void)__hip_atomic_exchange((GAS u32*)(ws + off), tag, RLX_AGENT); }
DI void publish(char* ws, u32 flagoff, u32 tag) {
  drain();
  bar();
  if (threadIdx.x == 0) setflag(ws, flagoff, tag);
}
DI void poll_n(char* ws, u32 off, int n, u32 tag, int lane) {
  GAS u32* f = (GAS u32*)(ws + off) + lane;
#pragma unroll 1
  for (u32 spin = 0; spin < SPIN_CAP; ++spin) {
    u32 v = tag;
    if (lane < n) { v = 0; asm volatile("global_atomic_or %0, %1, %0, off sc0 sc1\n\ts_waitcnt vmcnt(0)" : "+v"(v) : "v"(f) : "memory"); }
    if (__all(v == tag)) break;
    __builtin_amdgcn_s_sleep(1);
  }
}
DI void lds_post(u32 a, u32 tag) { asm volatile("" ::: "memory"); *(volatile LAS u32*)(size_t)a = tag; }
DI void lds_wait(u32 a, u32 tag) {
#pragma unroll 1
  for (u32 spin = 0; spin < (SPIN_CAP << 4); ++spin) {
    if (__all(*(volatile LAS u32*)(size_t)a == tag)) break;
    __builtin_amdgcn_s_sleep(2);
  }
  asm volatile("" ::: "memory");
}
DI void gather2(rsrc_t rs, u32 o1, u32 o2, u32 tag, v4u& g1, v4u& g2) {
#pragma unroll 1
  for (u32 spin = 0; spin < SPIN_CAP; ++spin) {
    asm volatile("" ::: "memory");
    g1 = ldb128c(rs, o1, 0);
    g2 = ldb128c(rs, o2, 0);
    if (__all(g1[1] == tag && g1[3] == tag && g2[1] == tag && g2[3] == tag)) break;
    __builtin_amdgcn_s_sleep(1);
  }
}
DI void gather4(rsrc_t rs, u32 o, u32 st, u32 tag, v4u& g0, v4u& g1, v4u& g2, v4u& g3) {
#pragma unroll 1
  for (u32 spin = 0; spin < SPIN_CAP; ++spin) {
    asm volatile("" ::: "memory");
    g0 = ldb128c(rs, o, 0); g1 = ldb128c(rs, o, st); g2 = ldb128c(rs, o, 2 * st); g3 = ldb128c(rs, o, 3 * st);
    if (__all(g0[1] == tag && g0[3] == tag && g1[1] == tag && g1[3] == tag && g2[1] == tag && g2[3] == tag && g3[1] == tag && g3[3] == tag)) break;
    __builtin_amdgcn_s_sleep(1);
  }
}
DI v2f sum4(const v4u& a, const v4u& b, const v4u& c, const v4u& d, float one) {
  const float x = (__uint_as_float(a[0]) + __uint_as_float(b[0])) + (__uint_as_float(c[0]) + __uint_as_float(d[0]));
  const float y = (__uint_as_float(a[2]) + __uint_as_float(b[2])) + (__uint_as_float(c[2]) + __uint_as_float(d[2]));
  return v2f{rbf(one + rbf(x)), rbf(one + rbf(y))};
}
DI bool tags4(const v4u& a, const v4u& b, const v4u& c, const v4u& d, u32 t) {
  return __all(a[1] == t && a[3] == t && b[1] == t && b[3] == t && c[1] == t && c[3] == t && d[1] == t && d[3] == t);
}
DI v4f unp4(v4u g, float one) { return v4f{rbf(one + bflo(g[0])), rbf(one + bfhi(g[0])), rbf(one + bflo(g[2])), rbf(one + bfhi(g[2]))}; }

DI void softmax20(const float (&p)[20], u32 (&o)[10]) {
  float mx = p[0];
#pragma unroll
  for (int i = 1; i < 20; ++i) mx = fmaxf(mx, p[i]);
  const float L2E = 1.4426950408889634f, a = -mx * L2E;
  float e[20], s0 = 0.f, s1 = 0.f;
#pragma unroll
  for (int i = 0; i < 20; ++i) { e[i] = __builtin_amdgcn_exp2f(__builtin_fmaf(p[i], L2E, a)); if (i & 1) s1 += e[i]; else s0 += e[i]; }
  const float r = __builtin_amdgcn_rcpf(s0 + s1);
#pragma unroll
  for (int i = 0; i < 10; ++i) o[i] = pkbf(e[2 * i] * r, e[2 * i + 1] * r);
}
__global__ __launch_bounds__(512, 2) void fused_kernel(const u16* __restrict__ x, const u16* __restrict__ wqkv, const u16* __restrict__ wphi,
                                                       const u16* __restrict__ wo, const u16* __restrict__ wg, const u16* __restrict__ wu,
                                                       const u16* __restrict__ wd, const u16* __restrict__ cvec, const u16* __restrict__ wmod,
                                                       u16* __restrict__ out, char* ws) {
  const int tid = threadIdx.x, lane = tid & 63;
  const int wave = __builtin_amdgcn_readfirstlane(tid >> 6);
  const int b = blockIdx.x, xcd = b & 7, idx = b >> 3;
  __builtin_assume(wave >= 0 && wave < 8);
  __builtin_assume(b >= 0 && b < 256);
  {
    const rsrc_t rs_cd = mkrs((const void*)(__builtin_amdgcn_s_getpc() & ~(u64)127), CODE_PF);
    asm volatile("s_mov_b32 m0, 0\n\ts_mov_b64 exec, 3\n\tbuffer_load_dword %0, %1, %2 offen lds\n\ts_mov_b64 exec, -1" ::"v"(lane * 32768), "s"(rs_cd), "s"((wave * 32 + idx) * 128) : "memory");
  }
  SB();
  STAMP(0);
  const int T4 = wave & 3, K2 = wave >> 2;
  const int mc = b >> 2, mk = b & 3;
  const u32 coff2 = (160 * mk + 40 * T4 + 5 * (lane >> 3)) * 2;
  const int P = 4 * (xcd >> 1) + (idx >> 3);
  const int J = 8 * (xcd & 1) + (idx & 7);
  const int TH = J >> 3, CT = J & 7;
  const rsrc_t rs_ws = mkrs(ws, WS_END);
  const rsrc_t rs_x = mkrs(x, SEQ * DM * 2);
  const u32 vlin = lane * 16;
  const int hh = lane >> 5, i32 = lane & 31, g4 = lane >> 4, t16 = lane & 15;
  const int g2 = g4 & 1, q4 = t16 >> 2, p4 = lane & 3;
  v4u wm[5], cq, tagq;
  {
    const rsrc_t rs_a = mkrs(K2 ? x : wmod, K2 ? SEQ * DM * 2 : DM * 6 * DM * 2), rs_c = mkrs(cvec, DM * 2);
    const u32 vo = K2 ? (128 * P + 8 * J + 2 * T4 + hh) * 1280 + i32 * 16 : coff2 * 3840 + mc * 128 + (lane & 7) * 16;
    aload<0>(cq, rs_c, coff2 & ~3u, 0);
    sfor<0, 5>([&](auto I) { aload<0>(wm[I.v], rs_a, vo, K2 ? (I.v < 3 ? I.v * 512 : 0x10000000) : I.v * 7680); });
    aloadc<0>(tagq, rs_ws, WS_EPOCH, 0);
  }
  SB();
  ldsw128(L_SYNC + (lane & 1) * 16, v4u{0u, 0u, 0u, 0u});
  ldsw128(L_ZERO, v4u{0u, 0u, 0u, 0u});
  const int rot32 = (idx & 7) * 4 + (idx >> 3);
  int R20 = (rot32 * 20) >> 5;
  const int R4A = rot32 >> 1, R4B = (rot32 * 11) >> 5;
  auto wrap = [](int v, int m) { return v >= m ? v - m : v; };
  const int t0 = 7 * J - (J >> 2), nt = (J & 3) == 3 ? 6 : 7;
  const int rq27 = 2 * P + TH;
  const u32 wqs = WS_WQ + J * 153600;
  auto tile = [&](u32 src, u32 rstride, u32 dst, u32 so) {
    astorec(__builtin_bit_cast(v4u, trfrag(src + q4 * rstride, src + (4 + q4) * rstride)), rs_ws, dst, so);
  };
  const int l10 = lane / 10, c10 = lane - l10 * 10;
  const u32 v10 = l10 * 1280 + c10 * 16;
  const u32 raw2 = 32768 + wave * 5120;
  const int gw5 = wave - 3;
  auto rawloads = [&]() {
    const int o = 8 * rq27 + wave;
    int jj, oo;
    if (o < 128) { jj = o >> 3; oo = o & 7; } else if (o < 192) { jj = (o - 128) >> 2; oo = 8 + (o & 3); } else { const int hq = (o - 192) >> 1; jj = hq + hq / 3; oo = 12 + (o & 1); }
    const int wdrow = (7 * jj - (jj >> 2)) * 16 + oo * 8;
    const rsrc_t rs_d = mkrs(wd, FF * DM * 2);
    if (rq27 < 27) { dmam<40>(rs_d, v10, wdrow * 1280 + CT * 160, raw2); dmam<40>(rs_d, v10, (wdrow + 4) * 1280 + CT * 160, raw2 + 640); }
    if (wave >= 3) {
      const rsrc_t rs_g = mkrs(wg, DM * FF * 2), rs_u = mkrs(wu, DM * FF * 2);
      const int row = lane / 14; int ch = lane - row * 14; if (ch >= 2 * nt) ch -= 2;
      const u32 vg = row * 3456 + ch * 16, so = (40 * P + 8 * gw5) * 3456 + t0 * 32;
      dmam<56>(rs_g, vg, so, raw2 + 1280); dmam<56>(rs_g, vg, so + 4 * 3456, raw2 + 2176);
      dmam<56>(rs_u, vg, so, raw2 + 3072); dmam<56>(rs_u, vg, so + 4 * 3456, raw2 + 3968);
    }
  };
  auto tileops = [&]() {
    const int o = 8 * rq27 + wave;
    const u32 du = (o & 3) * 256 + t16 * 16, sod = WS_WD + CT * 276480 + (o >> 2) * 5120;
    tile(raw2 + g4 * 32 + p4 * 8, 160, rq27 < 27 ? du + g4 * 1024 : OOB, sod);
    const int oct = 5 * P + gw5;
    const u32 dg = (oct & 3) * 256 + t16 * 16, sog = WS_GU + J * 286720;
    if (wave >= 3) {
      const u32 sg2 = raw2 + 1280 + hh * 1792 + p4 * 16 + g2 * 8;
      tile(sg2, 224, dg + (oct >> 2) * 8192 + g4 * 1024, sog);
      tile(sg2 + 64, 224, dg + (oct >> 2) * 8192 + 4096 + g4 * 1024, sog);
      tile(sg2 + 128, 224, dg + 163840 + (oct >> 2) * 6144 + g4 * 1024, sog);
    }
    const bool isd = g4 >= 2;
    const u32 a = isd ? raw2 + 128 + p4 * 8 : raw2 + 1280 + g2 * 1792 + 192 + p4 * 8, rs2 = isd ? 160 : 224;
    const u32 dd = g4 == 3 ? OOB : isd ? (rq27 < 27 ? sod + du + 4096 : OOB) : wave >= 3 ? sog + dg + 163840 + (oct >> 2) * 6144 + 4096 + g2 * 1024 : OOB;
    tile(a, rs2, dd, 0);
  };
  constexpr u32 L_WR = 140800, L_OR = L_WR - 6400;
  u32 ta[2] = {}, td[2][2] = {};
  if (K2) {
    {
      const rsrc_t rs_w = mkrs(wqkv, DM * 3 * DM * 2), rs_2 = mkrs(T4 == 1 ? wphi : wqkv, T4 == 1 ? 32 * 400 * 2 : DM * 3 * DM * 2);
      const int r = lane / 15, c = lane - r * 15, pt = c / 5;
      const u32 vo = r * 3840 + pt * 1280 + (c - pt * 5) * 16, so = (40 * P + 8 * T4) * 3840 + J * 80;
      dmam<60>(rs_w, vo, so, L_WR + T4 * 1920); dmam<60>(rs_w, vo, so + 4 * 3840, L_WR + T4 * 1920 + 960);
      const u32 v2 = T4 == 1 ? vlin : vo, s2 = T4 == 1 ? J * 1600 : (40 * P + 32) * 3840 + J * 80;
      dmam<60>(rs_2, v2, s2, T4 == 1 ? L_WPHI : L_WR + 7680); dmam<60>(rs_2, v2, s2 + (T4 == 1 ? 640 : 4 * 3840), T4 == 1 ? L_WPHI + 640 : L_WR + 8640);
    }
  {
    const u32 cm = (p4 >> 1) * 40 + (p4 & 1) * 16;
    ta[0] = opq(L_WR + q4 * 240 + 80 + hh * 80 + cm + g2 * 8);
    ta[1] = opq(L_WR + q4 * 240 + (g4 == 0 ? 80 + (p4 & 1) * 80 + (p4 >> 1) * 40 + 32 : g4 == 3 ? (p4 & 1) * 40 + 32 : cm + (g4 - 1) * 8));
    const u32 sA = g4 == 0 ? 5120 : 2560, sB = g4 == 3 ? 128 : 256, sC = (g4 == 0 ? 4096 : (g4 == 3 && t16 >= 8) ? OOB : 102400 + (g4 - 1) * 1024) + t16 * 16;
#pragma unroll
    for (int z = 0; z < 2; ++z) {
      const int oct = 5 * P + (z ? 4 : T4);
      td[z][0] = opq(wqs + (oct >> 2) * 5120 + (oct & 3) * 256 + g4 * 1024 + t16 * 16);
      td[z][1] = opq(wqs + (oct >> 2) * sA + (oct & 3) * sB + sC);
    }
  }
  }
  u32 tag;
  bar();
  await1<6>(cq);
  float sv[5];
  {
    const u32 sh = (coff2 & 2) * 8;
    const u32 ce[3] = {__builtin_amdgcn_alignbit(cq[1], cq[0], sh), __builtin_amdgcn_alignbit(cq[2], cq[1], sh), cq[2] >> sh};
#pragma unroll
    for (int i = 0; i < 5; ++i) {
      const float cv = (i & 1) ? bfhi(ce[i >> 1]) : bflo(ce[i >> 1]);
      sv[i] = rbf(cv * __builtin_amdgcn_rcpf(1.f + __builtin_amdgcn_exp2f(-1.4426950408889634f * cv)));
      asm volatile("" : "+v"(sv[i]));
    }
  }
  await1<0>(tagq);
  asm volatile("" : "+v"(wm[0]), "+v"(wm[1]), "+v"(wm[2]), "+v"(wm[3]), "+v"(wm[4]));
  tag = tagq[0] + 1u;
  WST(0);
  if (!K2) {
    float acc[8] = {0.f, 0.f, 0.f, 0.f, 0.f, 0.f, 0.f, 0.f};
#pragma unroll
    for (int i = 0; i < 5; ++i)
#pragma unroll
      for (int e = 0; e < 4; ++e) { acc[2 * e] += sv[i] * bflo(wm[i][e]); acc[2 * e + 1] += sv[i] * bfhi(wm[i][e]); }
    ldswf(L_KVB + T4 * 256 + ((lane & 7) * 8 + 4 * hh + 2 * g2 + ((lane >> 3) & 1)) * 4, red8(acc, lane));
    if (wave) lds_post(L_SYNC + 12 + wave * 4, 1u);
    else {
      lds_wait(L_SYNC + 16 + (lane % 3) * 4, 1u);
      const u32 a = L_KVB + lane * 4;
      const float v = (ldsrf(a) + ldsrf(a + 256)) + (ldsrf(a + 512) + ldsrf(a + 768));
      stb64c(rs_ws, b < 240 ? WS_MODG + (mk * 3840 + mc * 64 + lane) * 8 : OOB, v2u{__float_as_uint(v), tag});
      drain();
      const int vec = mc >= 10;
      if (mc < 20 && lane < 32 && ((40 * t16 + 39 * g2) >> 6) == mc - 10 * vec) setflag(ws, WS_FLAG + (6 * 16 + t16) * 512 + 256 + (vec * 8 + g2 * 4 + mk) * 4, tag);
    }
    WST(1);
    {
      const int r5 = lane / 5;
      const u32 vs = r5 * 1280 + (lane - r5 * 5) * 16, so = (128 * P + 32 * T4) * 1280 + J * 80, lx = L_RAW + T4 * 2880;
      dmam<60>(rs_x, vs, so, lx); dmam<60>(rs_x, vs, so + 12 * 1280, lx + 960); dmam<40>(rs_x, vs, so + 24 * 1280, lx + 1920);
    }
    if (wave == 2) {
      const int e = lane < 40 ? lane : 39, vec = e >= 20;
      v4u g0, g1, g2v, g3;
      poll_n(ws, WS_FLAG + (6 * 16 + J) * 512 + 256, 16, tag, lane);
      gather4(rs_ws, WS_MODG + (vec * 640 + 40 * J) * 8 + (e - 20 * vec) * 16, 30720, tag, g0, g1, g2v, g3);
      *(LAS v2f*)(size_t)(L_COA + e * 8) = sum4(g0, g1, g2v, g3, vec ? 1.f : 0.f);
      lds_post(L_SYNC, 1u);
    } else if (wave == 3) {
      v4u g0, g1;
      poll_n(ws, WS_FLAG + (5 * 16 + P) * 512 + 256, 64, tag, lane);
      gather2(rs_ws, WS_ST1 + P * 2048 + vlin, WS_ST1 + P * 2048 + 1024 + vlin, tag, g0, g1);
      ldsw64(L_COS + lane * 8, v2u{g0[0], g0[2]}); ldsw64(L_COS + 512 + lane * 8, v2u{g1[0], g1[2]});
      lds_post(L_SYNC + 4, 1u);
    }
    drain();
    v4u xs[3];
    xs[0] = ldsr128(L_RAW + T4 * 2880 + t16 * 80 + g4 * 16); xs[1] = ldsr128(L_RAW + T4 * 2880 + 1280 + t16 * 80 + g4 * 16); xs[2] = ldsr128(L_RAW + T4 * 2880 + i32 * 80 + 64);
    WST(4);
    lds_wait(L_SYNC + (lane & 1) * 4, 1u);
    const u32 cw = L_COA;
    const v2u s0 = ldsr64(L_COS + (32 * T4 + t16) * 8), s1 = ldsr64(L_COS + (32 * T4 + 16 + t16) * 8), s2 = g2 ? s1 : s0;
    auto hch = [&](const v4u& xv, u32 cb, u32 mu, u32 ru) {
      const float rs = __uint_as_float(ru), mr = -__uint_as_float(mu) * rs;
      v4u o;
#pragma unroll
      for (int z = 0; z < 2; ++z) {
        const v4f S = ldsr128f(cb + z * 16), A = ldsr128f(cb + 160 + z * 16);
        o[2 * z] = pkbf(__builtin_fmaf(__builtin_fmaf(bflo(xv[2 * z]), rs, mr), A[0], S[0]), __builtin_fmaf(__builtin_fmaf(bfhi(xv[2 * z]), rs, mr), A[1], S[1]));
        o[2 * z + 1] = pkbf(__builtin_fmaf(__builtin_fmaf(bflo(xv[2 * z + 1]), rs, mr), A[2], S[2]), __builtin_fmaf(__builtin_fmaf(bfhi(xv[2 * z + 1]), rs, mr), A[3], S[3]));
      }
      return o;
    };
    const u32 tb = WS_T + ((8 * P + 2 * T4) * 80 + 5 * J) * 256;
    stb128c(rs_ws, tb + vlin, hch(xs[0], cw + g4 * 32, s0[0], s0[1]));
    stb128c(rs_ws, tb + 20480 + vlin, hch(xs[1], cw + g4 * 32, s1[0], s1[1]));
    stb128c(rs_ws, lane < 32 ? tb + g2 * 20480 + 1024 + t16 * 16 : OOB, hch(xs[2], cw + 128, s2[0], s2[1]));
    drain();
    if (lane == 0) setflag(ws, WS_FLAG + (6 * 16 + P) * 512 + (T4 * 16 + J) * 4, tag);
    WST(3);
    STAMP(1);
  } else {
    {
      const u32 ro = T4 * 1920;
      const v8bf f0 = trfrag(ta[0] + ro, ta[0] + ro + 960), f1 = trfrag(ta[1] + ro, ta[1] + ro + 960);
      const u32 tz = (T4 >> 1) ? ta[T4 & 1] : ta[0];
      const v8bf f2 = trfrag(tz + 7680, tz + 8640);
      stb128c(rs_ws, td[0][0], __builtin_bit_cast(v4u, f0)); stb128c(rs_ws, td[0][1], __builtin_bit_cast(v4u, f1));
      stb128c(rs_ws, (T4 >> 1) ? ((T4 & 1) ? td[1][1] : td[1][0]) : OOB, __builtin_bit_cast(v4u, f2));
    }
    {
      float tn[24];
#pragma unroll
      for (int z = 0; z < 3; ++z)
#pragma unroll
        for (int e = 0; e < 4; ++e) { tn[8 * z + 2 * e] = bflo(wm[z][e]); tn[8 * z + 2 * e + 1] = bfhi(wm[z][e]); }
      auto hsum = [](float a) { return xsum16(dppadd<DPP_ROR8>(dppadd<DPP_ROR4>(dppadd<0x4e>(dppadd<0xb1>(a))))); };
      float sm = 0.f, sm2 = 0.f;
#pragma unroll
      for (int e = 0; e < 24; ++e) { if (e < 16) sm += tn[e]; else sm2 += tn[e]; }
      const float m = hsum(i32 < 16 ? sm + sm2 : sm) * (1.f / DM);
      float q = 0.f, q2 = 0.f;
#pragma unroll
      for (int e = 0; e < 24; ++e) { const float d = tn[e] - m; if (e < 16) q += d * d; else q2 += d * d; }
      const float rs = __builtin_amdgcn_rsqf(hsum(i32 < 16 ? q + q2 : q) * (1.f / DM) + 1e-5f);
      stb64c(rs_ws, i32 < 2 ? WS_ST1 + (128 * P + 8 * J + 2 * T4 + hh) * 16 + i32 * 8 : OOB, v2u{__float_as_uint(i32 ? rs : m), tag});
    }
    drain();
    if (lane < 2) setflag(ws, WS_FLAG + (lane ? (5 * 16 + J) * 512 + (P * 4 + T4) * 4 : (5 * 16 + P) * 512 + 256 + (J * 4 + T4) * 4), tag);
    WST(1);
    if (wave == 4) { poll_n(ws, WS_FLAG + (5 * 16 + J) * 512, 64, tag, lane); lds_post(L_SYNC + 8, 1u); }
    else {
      const rsrc_t rs_wo = mkrs(wo, DM * DM * 2);
      const int o0 = 2 * T4 - 2;
      const u32 so = (40 * P + 8 * o0) * 1280 + CT * 160, lr = L_OR + o0 * 1280;
      dmam<40>(rs_wo, v10, so, lr); dmam<40>(rs_wo, v10, so + 4 * 1280, lr + 640);
      if (T4 < 3) { dmam<40>(rs_wo, v10, so + 8 * 1280, lr + 1280); dmam<40>(rs_wo, v10, so + 12 * 1280, lr + 1920); }
      drain();
#pragma unroll 1
      for (int z = 0; z < 3; ++z) {
        const int ul = 4 * z + g4, hi = ul >= 5, tl = ul - 5 * hi, oct = 5 * P + o0 + hi;
        const bool ok = ul < (T4 < 3 ? 10 : 5);
        tile(lr + (ok ? hi * 1280 + tl * 32 : 0) + p4 * 8, 160, ok ? (oct >> 2) * 5120 + tl * 1024 + (oct & 3) * 256 + t16 * 16 : OOB, WS_WO + J * 102400);
      }
      lds_wait(L_SYNC + 8, 1u);
    }
    WST(3);
#pragma unroll 1
    for (int e = T4; e < 100; e += 4) dma16c(rs_ws, vlin, wqs + wrap(e + 5 * R20, 100) * 1024, e * 1024);
    WST(4);
  }
  WST(5);
  STAMP(2);
  if (wave == 0) poll_n(ws, WS_FLAG + (6 * 16 + P) * 512, 64, tag, lane);
  drain();
  bar();
  WST(6);
  STAMP(3);
  v4u hf[20];
  u32 lb[3];
  lb[0] = opq(vlin); lb[1] = opq(vlin + 51200); lb[2] = opq(vlin + 102400);
  auto frag = [&](u32 off) { return asbf(ldsr128(lb[off / 51200] + off % 51200)); };
  v4f aC[5] = {};
  v4u wpf[5][2];
  {
    constexpr int H0 = 8;
    const u32 hvo = WS_T + (8 * P + wave) * 20480 + vlin;
    auto ldh = [&](auto N) { aloadc<0>(hf[N.v], rs_ws, hvo, wrap(N.v + R20, 20) * 1024); };
    sfor<0, H0>(ldh);
  {
    const u32 mk0 = (q4 >> 1) == hh ? 0xffffffffu : 0u, mk1 = g2 ? 0u : mk0, sgn = (q4 & 1) ? 0x80008000u : 0u;
    const u32 r0 = L_WPHI + hh * 800 + q4 * 40;
#pragma unroll
    for (int T = 0; T < 5; ++T) {
      const u32 c = ((p4 & 1) ? (T + 1) % 5 : T) * 8;
      wpf[T][0] = __builtin_bit_cast(v4u, trfrag(r0 + g2 * 320 + c, r0 + g2 * 320 + 160 + c));
      const v2u l = __builtin_bit_cast(v2u, trd(r0 + 640 + c));
#pragma unroll
      for (int e = 0; e < 4; ++e) wpf[T][0][e] = (wpf[T][0][e] ^ sgn) & mk0;
      wpf[T][1] = v4u{(l[0] ^ sgn) & mk1, (l[1] ^ sgn) & mk1, 0u, 0u};
      asm volatile("" : "+v"(wpf[T][0]), "+v"(wpf[T][1]));
    }
  }
    v8bf wf[10];
    sfor<0, 10>([&](auto F) { wf[F.v] = frag(F.v * 1024); });
    SB();
    sfor<0, 20>([&](auto N) {
      constexpr int n = N.v;
      await1<7>(hf[n]);
      sfor<0, 5>([&](auto T) {
        constexpr int t = T.v, f = (5 * n + t) % 10;
        aC[t] = mfma16(wf[f], asbf(hf[n]), aC[t]);
        if constexpr (n < 18) wf[f] = frag((5 * n + 10 + t) * 1024);
        if constexpr (t == 0 && n < 12) ldh(IC<(n < 12 ? n + H0 : 0)>{});
        if constexpr (t == 0 && n >= 12 && n < 19) {
          const int e = n < 18 ? 8 * (n - 12) + wave : 48 + (wave & 1);
          dma16c(rs_ws, vlin, wqs + 102400 + wrap(e + 5 * (R20 >> 1), 50) * 1024, L_RQ + e * 1024);
        }
        SB();
      });
    });
    asm volatile("" : "+v"(aC[0]), "+v"(aC[1]), "+v"(aC[2]), "+v"(aC[3]), "+v"(aC[4]));
  }
  WST(7);
  STAMP(4);
  auto phi = [&](const v4f& m0, const v4f& m1, const v4f& l, u32 (&o)[10]) {
    const v4u b0 = {pkbf(m0[0], m0[1]), pkbf(m0[2], m0[3]), pkbf(m1[0], m1[1]), pkbf(m1[2], m1[3])};
    const v4u b1 = {pkbf(l[0], l[1]), pkbf(l[2], l[3]), 0u, 0u};
    float pv[20];
#pragma unroll
    for (int T = 0; T < 5; ++T) {
      v4f d = {};
      d = mfma16(asbf(wpf[T][0]), asbf(b0), d); d = mfma16(asbf(wpf[T][1]), asbf(b1), d);
#pragma unroll
      for (int e = 0; e < 4; ++e) pv[4 * T + e] = d[e];
    }
    softmax20(pv, o);
  };
  {
    u32 pk[10];
    phi(aC[0], aC[1], aC[4], pk);
    const int tok = 16 * wave + t16;
    const u32 fx = (((tok >> 1) & 1) << 2) | ((tok & 1) << 1) | ((tok >> 2) & 1), rb = hh * 16384 + tok * 128;
    auto cad = [&](int cc) { return rb + ((cc ^ fx) << 4); };
    bar();
    ldsw128(cad(g2), v4u{pkbf(aC[2][0], aC[2][1]), pkbf(aC[2][2], aC[2][3]), pkbf(aC[3][0], aC[3][1]), pkbf(aC[3][2], aC[3][3])});
    if (g2) ldsw128(cad(2), v4u{pkbf(aC[4][0], aC[4][1]), pkbf(aC[4][2], aC[4][3]), 0x00003f80u, 0u});
    ldsw128(cad(g2 ? 7 : 4), v4u{pk[0], pk[1], pk[2], pk[3]});
    ldsw128(cad(g2 ? 3 : 5), v4u{pk[4], pk[5], pk[6], pk[7]});
    ldsw64(cad(6) + g2 * 8, v2u{pk[8], pk[9]});
  }
  drain();
  bar();
  STAMP(5);
  const bool dog2 = b < 216;
  const int g2c = b % 54, g2k = b / 54;
  const int tokE = 128 * P + 64 * TH + 16 * T4 + t16;
  v4u gw[5], xr[3], gq[4];
  const int ge = lane < 40 ? lane : 39;
  const u32 gof = WS_MODG + (wave == 4 ? 1920 + (g2k & 3) * 160 + 2 * lane : wave == 5 ? 2560 + (g2k & 3) * 160 + 2 * lane
                          : wave == 6 ? (lane < 40 ? 1280 + CT * 80 + 2 * lane : lane < 56 ? 1920 + (g2k & 3) * 160 + 2 * (lane + 24) : 2560 + (g2k & 3) * 160 + 2 * (lane + 8))
                          : wave == 7 ? (lane < 40 ? 3200 + CT * 80 + 2 * lane : 2560 + (g2k & 3) * 160 + 2 * (72 + (lane & 7)))
                                      : 2560 + CT * 80 + 2 * ge) * 8;
  const u32 gld = wave == 4 ? L_G2S + lane * 8 : wave == 5 ? L_G2A + lane * 8
                : wave == 6 ? (lane < 40 ? L_WPHI + lane * 8 : lane < 56 ? L_G2S + (lane + 24) * 8 : L_G2A + (lane + 8) * 8)
                : wave == 7 ? (lane < 40 ? L_WPHI + 320 + lane * 8 : L_G2A + (72 + (lane & 7)) * 8) : L_WPHI + 640 + ge * 8;
  const float gone = (wave == 5 || wave < 4 || (wave == 6 && lane >= 56) || (wave == 7 && lane >= 40)) ? 1.f : 0.f;
  if (wave < 4) {
    const int hd = wave >> 1, tl = wave & 1;
    u32 av[2], ap[2];
#pragma unroll
    for (int t = 0; t < 2; ++t) {
      const int tok = 8 * hh + 4 * t + q4;
      const u32 fx = (((tok >> 1) & 1) << 2) | ((tok & 1) << 1) | ((tok >> 2) & 1), cl = 2 * g2 + (p4 >> 1);
      av[t] = hd * 16384 + tok * 128 + ((cl ^ fx) << 4) + 8 * (p4 & 1);
      ap[t] = hd * 16384 + tok * 128 + ((((tl ? 0 : 4) + cl) ^ fx) << 4) + 8 * (p4 & 1);
    }
    v8bf fv[8], fp[8];
#pragma unroll
    for (int s = 0; s < 8; ++s) { fp[s] = trfrag(ap[0] + s * 2048, ap[1] + s * 2048); fv[s] = trfrag(av[0] + s * 2048, av[1] + s * 2048); }
    v16f d = {};
#pragma unroll
    for (int s = 0; s < 8; ++s) d = mfma32(fp[s], fv[s], d);
    const u32 base = i32 < 21 ? WS_KVP + (J * 16 + P) * KVP_STRIDE + hd * 3360 + i32 * 16 + hh * 336 : OOB;
    if (tl == 0) {
#pragma unroll
      for (int rq = 0; rq < 3; ++rq) stb128c(rs_ws, base + rq * 672, __builtin_bit_cast(v4u, v4f{d[4 * rq], d[4 * rq + 1], d[4 * rq + 2], d[4 * rq + 3]}));
    }
    stb128c(rs_ws, base + (3 + tl) * 672, __builtin_bit_cast(v4u, v4f{d[12], d[13], d[14], d[15]}));
    drain();
    if (lane == 0) setflag(ws, WS_FLAG + (0 * 16 + J) * 512 + (P * 4 + wave) * 4, tag);
    STAMP(6);
  }
  rawloads();
  WST(8);
  auto wo1 = [&](int e) { dma16c(rs_ws, vlin, WS_WO + J * 102400 + e * 1024, e * 1024); };
  u32 pq[10];
  {
    v4f aQ[3] = {};
    const int odd = R20 & 1;
    const u32 bq = opq(L_RQ + odd * 2560 + vlin), bl = opq(L_RQ + odd * 2560 + 2048 + g4 * 128 + ((t16 >> 3) * 4 + p4) * 16);
    const u32 bq19 = bq + (odd ? -2560 : 19 * 2560), bl19 = bl + (odd ? -2560 : 19 * 2560);
    auto qf = [&](int n, int t) { return asbf(ldsr128(n == 19 ? (t == 2 ? bl19 : bq19 + t * 1024) : (t == 2 ? bl + n * 2560 : bq + n * 2560 + t * 1024))); };
    v8bf wf[9];
#pragma unroll
    for (int f = 0; f < 9; ++f) wf[f] = qf(f / 3, f % 3);
    SB();
#pragma unroll
    for (int n = 0; n < 60; ++n) {
      aQ[n % 3] = mfma16(wf[n % 9], asbf(hf[n / 3]), aQ[n % 3]);
      if (n + 9 < 60) wf[n % 9] = qf((n + 9) / 3, n % 3);
      SB();
    }
    asm volatile("" : "+v"(aQ[0]), "+v"(aQ[1]), "+v"(aQ[2]));
    if (wave >= 3) { sfor<0, 4>([&](auto I) { gq[I.v] = ldb128c(rs_ws, gof, I.v * 30720); }); }
    if (wave >= 4) {
      const rsrc_t rs_gu = mkrs(g2c < 27 ? wg : wu, DM * FF * 2);
      const u32 vo = dog2 ? (160 * g2k + 40 * T4 + (lane >> 3)) * 3456 + (g2c < 27 ? g2c : g2c - 27) * 128 + (lane & 7) * 16 : OOB;
#pragma unroll
      for (int i = 0; i < 5; ++i) gw[i] = ldb128(rs_gu, vo, i * 8 * 3456);
#pragma unroll
      for (int i = 0; i < 3; ++i) { const int it = i * 64 + lane < 160 ? i * 64 + lane : 159, tk = it / 10; xr[i] = ldb128(rs_x, (128 * P + 64 * TH + 16 * T4 + tk) * 1280 + (it - tk * 10) * 16, CT * 160); }
    }
    SB();
    phi(aQ[0], aQ[1], aQ[2], pq);
#pragma unroll
    for (int i = 0; i < 10; ++i) asm volatile("" : "+v"(pq[i]));
  }
  if (wave >= 3) {
    if (!tags4(gq[0], gq[1], gq[2], gq[3], tag)) gather4(rs_ws, gof, 30720, tag, gq[0], gq[1], gq[2], gq[3]);
    *(LAS v2f*)(size_t)gld = sum4(gq[0], gq[1], gq[2], gq[3], gone);
  }
  drain();
  tileops();
  WST(9);
  if (wave == 7) poll_n(ws, WS_FLAG + (0 * 16 + J) * 512, 64, tag, lane);
  STAMP(7);
  bar();
  STAMP(8);
  {
    v4u pv[16];
    const u32 c16 = tid < 420 ? tid * 16 : OOB;
    sfor<0, 16>([&](auto I) { aloadc<0>(pv[I.v], rs_ws, c16, WS_KVP + (J * 16 + I.v) * KVP_STRIDE); });
    await4<12>(pv[0], pv[1], pv[2], pv[3]);
    v4f sum = (__builtin_bit_cast(v4f, pv[0]) + __builtin_bit_cast(v4f, pv[1])) + (__builtin_bit_cast(v4f, pv[2]) + __builtin_bit_cast(v4f, pv[3]));
    sfor<1, 4>([&](auto G) {
      constexpr int g = G.v;
      await4<12 - 4 * g>(pv[4 * g], pv[4 * g + 1], pv[4 * g + 2], pv[4 * g + 3]);
      sum += (__builtin_bit_cast(v4f, pv[4 * g]) + __builtin_bit_cast(v4f, pv[4 * g + 1])) + (__builtin_bit_cast(v4f, pv[4 * g + 2]) + __builtin_bit_cast(v4f, pv[4 * g + 3]));
    });
    const int c = tid, hd = c >= 210, rem = c - hd * 210, fq = rem / 21, d = rem - fq * 21;
    if (tid < 420) ldsw64(L_KVB + hd * 1680 + d * 80 + fq * 8, v2u{pkbf(sum[0], sum[1]), pkbf(sum[2], sum[3])});
  }
  WST(10);
  sfor<0, WO_EARLY>([&](auto I) { wo1(8 * I.v + wave); });
  bar();
  STAMP(9);
  {
    const bool act = (t16 >> 3) == hh;
    const int hf2 = q4 & 1;
    const u32 kb = L_KVB + hh * 1680;
    const u32 a01 = kb + (4 * hh + 8 * hf2 + p4) * 80, a2 = kb + (hf2 ? 20 : (hh ? 0 : 16) + p4) * 80;
    v4f d[3] = {};
    const v4u b0 = {pq[0], pq[1], pq[2], pq[3]}, b1 = {pq[4], pq[5], pq[6], pq[7]}, b2 = {pq[8], pq[9], 0u, 0u};
#pragma unroll
    for (int T = 0; T < 3; ++T) {
      const u32 r = T < 2 ? a01 + T * 320 : a2;
      const v4u k0 = ldsr128(act ? r + g2 * 48 : L_ZERO), k1 = ldsr128(act ? r + 16 + g2 * 48 : L_ZERO);
      const v2u k2 = ldsr64(act ? r + 32 + g2 * 8 : L_ZERO);
      d[T] = mfma16(asbf(k0), asbf(b0), d[T]); d[T] = mfma16(asbf(k1), asbf(b1), d[T]); d[T] = mfma16(asbf(v4u{k2[0], k2[1], 0u, 0u}), asbf(b2), d[T]);
    }
    const auto dn = __builtin_amdgcn_permlane16_swap(__float_as_uint(d[2][0]), __float_as_uint(d[2][0]), false, false);
    const float rd = 1.0f / (__uint_as_float(dn[1]) + 1e-6f);
    const u32 ob = WS_O + ((8 * P + wave) * 80 + 5 * J) * 256 + t16 * 16;
    stb128c(rs_ws, ob + (3 * hh + g2) * 256, v4u{pkbf(d[0][0] * rd, d[0][1] * rd), pkbf(d[0][2] * rd, d[0][3] * rd), pkbf(d[1][0] * rd, d[1][1] * rd), pkbf(d[1][2] * rd, d[1][3] * rd)});
    stb64c(rs_ws, g2 ? OOB : ob + 512 + hh * 8, v2u{pkbf(d[2][0] * rd, d[2][1] * rd), pkbf(d[2][2] * rd, d[2][3] * rd)});
  }
  publish(ws, WS_FLAG + (1 * 16 + P) * 512 + J * 4, tag);
  if (tid == 64) setflag(ws, WS_FLAG + (2 * 16 + CT) * 512 + 256 + rq27 * 4, tag);
  STAMP(10);
  R20 = sopq(R20);
  const u32 gus = WS_GU + J * 286720;
  if (wave >= 1 && wave < 4) {
#pragma unroll 1
    for (int e = 8 * WO_EARLY + wave - 1; e < 100; e += 3) { wo1(e); waitv<GU_THR>(); }
    if (wave == 3) poll_n(ws, WS_FLAG + (2 * 16 + CT) * 512 + 256, 32, tag, lane);
    drain();
  }
  v2u xq[5];
  if (wave >= 4) {
    const u32 xb = 102400 + T4 * 2816;
#pragma unroll
    for (int i = 0; i < 3; ++i) { const int it = i * 64 + lane < 160 ? i * 64 + lane : 159, tk = it / 10; ldsw128(xb + tk * 176 + (it - tk * 10) * 16, xr[i]); }
#pragma unroll
    for (int t = 0; t < 5; ++t) { xq[t] = ldsr64(xb + t16 * 176 + t * 32 + g4 * 8); asm volatile("" : "+v"(xq[t])); }
    float sa[8] = {}, sb2[8] = {};
#pragma unroll
    for (int i = 0; i < 5; ++i) {
      const float ca = ldsrf(L_G2A + (40 * T4 + 8 * i + (lane >> 3)) * 4), cs = ldsrf(L_G2S + (40 * T4 + 8 * i + (lane >> 3)) * 4);
#pragma unroll
      for (int e = 0; e < 4; ++e) {
        const float lo = bflo(gw[i][e]), hi = bfhi(gw[i][e]);
        sa[2 * e] += ca * lo; sa[2 * e + 1] += ca * hi; sb2[2 * e] += cs * lo; sb2[2 * e + 1] += cs * hi;
      }
    }
#pragma unroll
    for (int e = 0; e < 8; ++e) { sa[e] = xsum32(xsum16(dppadd<DPP_ROR8>(sa[e]))); sb2[e] = xsum32(xsum16(dppadd<DPP_ROR8>(sb2[e]))); }
    if (lane < 8) {
      const u32 a = L_G2R + T4 * 512 + lane * 32;
      ldsw128f(a, v4f{sa[0], sa[1], sa[2], sa[3]}); ldsw128f(a + 16, v4f{sa[4], sa[5], sa[6], sa[7]});
      ldsw128f(a + 256, v4f{sb2[0], sb2[1], sb2[2], sb2[3]}); ldsw128f(a + 272, v4f{sb2[4], sb2[5], sb2[6], sb2[7]});
    }
  }
  SB();
  WST(11);
  if (wave == 0) poll_n(ws, WS_FLAG + (1 * 16 + P) * 512, 16, tag, lane);
  bar();
  STAMP(11);
  auto gu1 = [&](int e8) {
    const u32 src = e8 < 160 ? wrap(e8 + 8 * R20, 160) + wave : 160 + wrap(e8 - 160 + wave + 6 * R20, 120);
    dma16c(rs_ws, vlin, gus + src * 1024, ((e8 + 104) % 144 + wave) * 1024);
  };
  u32 kb2[3];
  kb2[0] = opq(vlin + K2 * 5120); kb2[1] = opq(vlin + K2 * 5120 + 51200); kb2[2] = opq(vlin + K2 * 5120 + 102400);
  auto frag2 = [&](u32 off) { return asbf(ldsr128(kb2[off / 51200] + off % 51200)); };
  float x1[20];
  {
    v4u of[10];
    const u32 ab = WS_O + ((8 * P + 4 * TH + T4) * 80 + 4 * K2) * 256 + vlin;
    sfor<0, 10>([&](auto S) { aloadc<(S.v & 1) * 2048>(of[S.v], rs_ws, ab, (S.v >> 1) * 4096); });
    sfor<0, 5>([&](auto I) { gu1(8 * I.v); });
    if (dog2 && wave == 0) {
      const u32 a = L_G2R + lane * 4;
      const float av = (ldsrf(a) + ldsrf(a + 512)) + (ldsrf(a + 1024) + ldsrf(a + 1536));
      const float bv = (ldsrf(a + 256) + ldsrf(a + 768)) + (ldsrf(a + 1280) + ldsrf(a + 1792));
      stb64c(rs_ws, WS_AB + (g2k * 3456 + g2c * 64 + lane) * 16, v2u{__float_as_uint(av), tag});
      stb64c(rs_ws, WS_AB + (g2k * 3456 + g2c * 64 + lane) * 16 + 8, v2u{__float_as_uint(bv), tag});
    }
    v8bf wf[10];
    sfor<0, 10>([&](auto F) { wf[F.v] = frag2((F.v / 5) * 10240 + (F.v % 5) * 1024); });
    v4f acc[5] = {};
    SB();
    sfor<0, 10>([&](auto S) {
      constexpr int s = S.v;
      await1<14 - s>(of[s]);
      sfor<0, 5>([&](auto T) {
        constexpr int t = T.v, f = (5 * s + t) % 10;
        acc[t] = mfma16(wf[f], asbf(of[s]), acc[t]);
        if constexpr (s < 8) wf[f] = frag2((s + 2) * 10240 + t * 1024);
        SB();
      });
    });
    asm volatile("" : "+v"(acc[0]), "+v"(acc[1]), "+v"(acc[2]), "+v"(acc[3]), "+v"(acc[4]));
    WST(12);
    bar();
    const u32 xb = 81920 + T4 * 5120 + vlin;
    if (K2 == 0) {
#pragma unroll
      for (int t = 0; t < 5; ++t) ldsw128f(xb + t * 1024, acc[t]);
    }
    bar();
    if (K2 == 1) {
      float sum = 0.f;
#pragma unroll
      for (int t = 0; t < 5; ++t) {
        const v4f a = acc[t] + ldsr128f(xb + t * 1024);
        const v4f ga = ldsr128f(L_WPHI + t * 64 + g4 * 16), A2 = ldsr128f(L_WPHI + 640 + t * 64 + g4 * 16);
        const u32 p0 = pkbf(bflo(xq[t][0]) + ga[0] * a[0], bfhi(xq[t][0]) + ga[1] * a[1]);
        const u32 p1 = pkbf(bflo(xq[t][1]) + ga[2] * a[2], bfhi(xq[t][1]) + ga[3] * a[3]);
        x1[4 * t] = bflo(p0); x1[4 * t + 1] = bfhi(p0); x1[4 * t + 2] = bflo(p1); x1[4 * t + 3] = bfhi(p1);
        sum += (x1[4 * t] + x1[4 * t + 1]) + (x1[4 * t + 2] + x1[4 * t + 3]);
        const v2u uu = {pkbf(x1[4 * t] * A2[0], x1[4 * t + 1] * A2[1]), pkbf(x1[4 * t + 2] * A2[2], x1[4 * t + 3] * A2[3])};
        stb64c(rs_ws, WS_U + ((tokE >> 4) * 80 + CT * 10 + 2 * t + hh) * 256 + t16 * 16 + g2 * 8, uu);
      }
      const float mt = xsum32(xsum16(sum)) * (1.f / 80.f);
      float m2 = 0.f;
#pragma unroll
      for (int i = 0; i < 20; ++i) { const float dd = x1[i] - mt; m2 += dd * dd; }
      m2 = xsum32(xsum16(m2));
      stb64c(rs_ws, g4 == 0 ? WS_STAT + ((P * 8 + CT) * 128 + (tokE & 127)) * 8 : OOB, v2u{__float_as_uint(mt), __float_as_uint(m2)});
      drain();
      if (lane == 0) setflag(ws, WS_FLAG + (2 * 16 + P) * 512 + (J * 4 + T4) * 4, tag);
#pragma unroll 1
      for (int z = 0; z < 6; ++z) { const int e = z < 5 ? 120 + 5 * T4 + z : 140 + T4; dma16c(rs_ws, vlin, gus + wrap(e + 8 * R20, 160) * 1024, (e - 40) * 1024); }
    } else {
#pragma unroll
      for (int i = 0; i < 20; ++i) x1[i] = 0.f;
      if (wave > 0) {
#pragma unroll 1
        for (int e = 39 + wave; e < 120; e += 3) { dma16c(rs_ws, vlin, gus + wrap(e + 8 * R20, 160) * 1024, (e - 40) * 1024); waitv<GU_THR>(); }
      }
    }
  }
  STAMP(12);
  WST(13);
  const u32 wds = WS_WD + CT * 276480;
  auto wd1 = [&](int e) { dma16c(rs_ws, vlin, wds + (e < 160 ? wrap(e + 10 * R4A, 160) : 160 + wrap(e - 160 + 10 * R4B, 110)) * 1024, wrap(wrap(e + 100, 150), 150) * 1024); };
  {
    if (wave == 0) poll_n(ws, WS_FLAG + (2 * 16 + P) * 512, 64, tag, lane);
    bar();
    STAMP(13);
    R20 = sopq(R20);
    v4u uf[20], abg[4], st;
    const u32 uvo = WS_U + (8 * P + wave) * 20480 + vlin;
    constexpr int U0 = 8;
    auto ldu = [&](auto N) { aloadc<0>(uf[N.v], rs_ws, uvo, wrap(N.v + R20, 20) * 1024); };
    sfor<0, U0>(ldu);
    const int abe = T4 * 56 + (lane < 56 ? lane : 55);
    u32 aboff;
    { const int mat = abe >= 112, c = abe - 112 * mat; aboff = K2 ? OOB : WS_AB + (mat * 1728 + (t0 * 16 + c < 1728 ? t0 * 16 + c : 1727)) * 16; }
    const u32 stoff = WS_STAT + ((P * 8 + 2 * g4 + (lane & 1)) * 128 + 16 * wave + (t16 & ~1)) * 8;
    constexpr auto sl = [](int e) { return (u32)(((e + 104) % 144) * 1024); };
    v8bf wf[8];
    sfor<0, 8>([&](auto F) { wf[F.v] = frag(sl(F.v)); });
    v4f aA[8] = {}, aB[6] = {};
    constexpr auto nops = [](int m) { return (m < 17 ? 1 : 0) + (m >= 8 ? 1 : 0); };
    constexpr auto sum = [=](int a, int b2) { int c = 0; for (int m = a; m < b2; ++m) c += nops(m); return c; };
    SB();
    sfor<0, 20>([&](auto N) {
      constexpr int n = N.v;
      if constexpr (n == 2 || n == 8) { rawbar(); SB(); }
      if constexpr (n == 15) { waitv<sum(11, 15)>(); rawbar(); SB(); }
      await1<(n < U0 ? U0 - 1 - n + sum(0, n) : nops(n - U0) - 1 + sum(n - U0 + 1, n))>(uf[n]);
      sfor<0, 8>([&](auto T) {
        constexpr int t = T.v;
        aA[t] = mfma16(wf[t], asbf(uf[n]), aA[t]);
        wf[t] = frag(sl(8 * n + 8 + t));
        if constexpr (t == 0) {
          if constexpr (n + U0 < 20) ldu(IC<(n + U0 < 20 ? n + U0 : 0)>{});
          else if constexpr (n < 16) aloadc<0>(abg[n < 16 ? n - 12 : 0], rs_ws, aboff, (n - 12) * 3456 * 16);
          else if constexpr (n == 16) aloadc<0>(st, rs_ws, stoff, 0);
        }
        if constexpr (t == 4 && n >= 8) gu1(144 + 8 * (n - 8));
        SB();
      });
    });
    asm volatile("" : "+v"(aA[0]), "+v"(aA[1]), "+v"(aA[2]), "+v"(aA[3]), "+v"(aA[4]), "+v"(aA[5]), "+v"(aA[6]), "+v"(aA[7]));
    STAMP(14);
    await4<1>(abg[0], abg[1], abg[2], abg[3]);
    asm volatile("" : "+v"(st));
    if (wave < 4) {
      if (!tags4(abg[0], abg[1], abg[2], abg[3], tag)) gather4(rs_ws, aboff, 3456 * 16, tag, abg[0], abg[1], abg[2], abg[3]);
      ldswf(L_AB + abe * 4, (__uint_as_float(abg[0][0]) + __uint_as_float(abg[1][0])) + (__uint_as_float(abg[2][0]) + __uint_as_float(abg[3][0])));
      ldswf(L_AB + 896 + abe * 4, (__uint_as_float(abg[0][2]) + __uint_as_float(abg[1][2])) + (__uint_as_float(abg[2][2]) + __uint_as_float(abg[3][2])));
    }
    bar();
    float rs2, mr2;
    {
      const bool od = lane & 1;
      const float ma = __uint_as_float(od ? st[2] : st[0]), qa = __uint_as_float(od ? st[3] : st[1]);
      const float mb = __uint_as_float(__builtin_amdgcn_update_dpp(0u, od ? st[0] : st[2], 0xb1, 0xf, 0xf, true));
      const float qb = __uint_as_float(__builtin_amdgcn_update_dpp(0u, od ? st[1] : st[3], 0xb1, 0xf, 0xf, true));
      const float m = xsum32(xsum16(ma + mb)) * 0.125f;
      const float q = qa + qb + 80.f * ((ma - m) * (ma - m) + (mb - m) * (mb - m));
      rs2 = __builtin_amdgcn_rsqf(xsum32(xsum16(q)) * (1.f / DM) + 1e-5f); mr2 = -m * rs2;
    }
    auto geglu4 = [&](const v4f& g, const v4f& u, u32 cb) {
      const v4f ag = ldsr128f(cb), au = ldsr128f(cb + 448), bg = ldsr128f(cb + 896), bu = ldsr128f(cb + 1344);
      float o[4];
#pragma unroll
      for (int e = 0; e < 4; ++e) {
        const float gv = __builtin_fmaf(g[e], rs2, __builtin_fmaf(ag[e], mr2, bg[e]));
        const float uv = __builtin_fmaf(u[e], rs2, __builtin_fmaf(au[e], mr2, bu[e]));
        const float z = gv * __builtin_fmaf(gv * gv, -0.044715f * 2.302208198f, -2.302208198f);
        o[e] = gv * uv * __builtin_amdgcn_rcpf(1.f + __builtin_amdgcn_exp2f(z));
      }
      return v2u{pkbf(o[0], o[1]), pkbf(o[2], o[3])};
    };
    const u32 abm = opq(L_AB + g4 * 32);
    const u32 ob = WS_ACT + (8 * P + wave) * 55296;
    auto epiA = [&]() {
#pragma unroll
      for (int bk = 0; bk < 2; ++bk) {
        const v2u lo = geglu4(aA[4 * bk], aA[4 * bk + 2], abm + bk * 128), hi = geglu4(aA[4 * bk + 1], aA[4 * bk + 3], abm + bk * 128 + 16);
        astorec(v4u{lo[0], lo[1], hi[0], hi[1]}, rs_ws, vlin, ob + (2 * J + bk) * 1024);
      }
    };
    auto loopB = [&](auto N0, auto N1) {
      sfor<6 * N0.v, 6 * N1.v>([&](auto I) {
        constexpr int i = I.v;
        aB[i % 6] = mfma16(wf[i % 8], asbf(uf[i / 6]), aB[i % 6]);
        if constexpr (i + 8 < 120) wf[i % 8] = frag(sl(160 + i + 8));
        if constexpr (i < 30 && i % 6 == 3) gu1(240 + 8 * (i / 6));
        if constexpr (i >= 30 && i < 48 && i % 6 == 3) { if constexpr (i < 42) wd1(8 * (i / 6 - 5) + wave); else wd1(16 + (wave & 3)); }
        if constexpr (i >= 60 && i % 6 == 3) wd1(20 + 8 * (i / 6 - 10) + wave);
        if constexpr (i >= 96 && i < 108 && i % 6 == 0) wd1(100 + 8 * (i / 6 - 16) + wave);
        SB();
      });
      asm volatile("" : "+v"(aB[0]), "+v"(aB[1]), "+v"(aB[2]), "+v"(aB[3]), "+v"(aB[4]), "+v"(aB[5]));
    };
    if (wave < 4) epiA();
    SB();
    loopB(IC<0>{}, IC<8>{});
    if (wave >= 4) epiA();
    SB();
    loopB(IC<8>{}, IC<10>{});
    waitv<2>();
    rawbar();
    STAMP(15);
    loopB(IC<10>{}, IC<15>{});
    waitv<5>();
    rawbar();
    if (tid == 0) setflag(ws, WS_FLAG + (3 * 16 + P) * 512 + J * 4, tag);
    loopB(IC<15>{}, IC<20>{});
    STAMP(16);
    rawbar();
    sfor<0, 4>([&](auto I) { wd1(116 + 8 * I.v + wave); });
    {
      const v2u lo = geglu4(aB[0], aB[2], abm + 256), hi = geglu4(aB[1], aB[3], abm + 272);
      astorec(v4u{lo[0], lo[1], hi[0], hi[1]}, rs_ws, vlin, ob + (32 + J) * 1024);
      const v2u hf4 = geglu4(aB[4], aB[5], L_AB + 384 + g4 * 16);
      __builtin_amdgcn_raw_buffer_store_b64(hf4, rs_ws, nt == 7 ? hh * 256 + t16 * 16 + g2 * 8 : OOB, ob + 48 * 1024 + (J - (J >> 2)) * 512, AUX_SC1);
    }
  }
  WST(14);
  if (wave == 0) poll_n(ws, WS_FLAG + (3 * 16 + P) * 512, 16, tag, lane);
  waitv<6>();
  rawbar();
  STAMP(17);
  {
    constexpr int A0 = 8;
    v4u af[27];
    const u32 ab = WS_ACT + ((8 * P + 4 * TH + T4) * 216 + 4 * K2) * 256 + vlin;
    auto lda = [&](auto N) { constexpr int n = N.v; aloadc<0>(af[n], rs_ws, ab, (n < 16 ? wrap(n + R4A, 16) : 16 + wrap(n - 16 + R4B, 11)) * 2048); };
    sfor<0, A0>(lda);
    constexpr auto sl = [](int n, int t) { return (u32)(((n + 10) % 15) * 10240 + t * 1024); };
    v8bf wf[10];
    sfor<0, 10>([&](auto F) { wf[F.v] = frag2(sl(F.v / 5, F.v % 5)); });
    v4f acc[5] = {};
    constexpr auto nops = [](int m) { return (m + A0 < 27 ? 1 : 0) + (m >= 2 && m < 18 ? 1 : 0); };
    constexpr auto sum = [=](int a, int b2) { int c = 0; for (int m = a; m < b2; ++m) c += nops(m); return c; };
    SB();
    sfor<0, 27>([&](auto N) {
      constexpr int n = N.v;
      if constexpr (n == 8) { if (wave == 0) poll_n(ws, WS_FLAG + (4 * 16 + P) * 512, 16, tag, lane); }
      if constexpr (n >= 2 && n <= 22 && (n & 1) == 0) { if constexpr (n >= 8) waitv<sum(n - 4, n)>(); rawbar(); SB(); }
      if constexpr (n == 2) {
        if (tid == 0) setflag(ws, WS_FLAG + (4 * 16 + P) * 512 + J * 4, tag);
      }
      await1<(n < A0 ? A0 - 1 - n + sum(0, n) : nops(n - A0) - 1 + sum(n - A0 + 1, n))>(af[n]);
      sfor<0, 5>([&](auto T) {
        constexpr int t = T.v, f = (5 * n + t) % 10;
        acc[t] = mfma16(wf[f], asbf(af[n]), acc[t]);
        if constexpr (n < 25) wf[f] = frag2(sl(n + 2, t));
        if constexpr (n + A0 < 27 && t == 0) lda(IC<(n + A0 < 27 ? n + A0 : 0)>{});
        if constexpr (n >= 2 && n < 18 && t == 2) { const int e = 148 + 8 * (n - 2) + wave; wd1(n == 17 ? (e < 270 ? e : 269) : e); }
        SB();
      });
      if constexpr (n == 7 || n == 1) asm volatile("" : "+v"(acc[0]), "+v"(acc[1]), "+v"(acc[2]), "+v"(acc[3]), "+v"(acc[4]));
    });
    asm volatile("" : "+v"(acc[0]), "+v"(acc[1]), "+v"(acc[2]), "+v"(acc[3]), "+v"(acc[4]));
    STAMP(18);
    const u32 xb = 81920 + T4 * 5120 + vlin;
    if (K2 == 0) {
#pragma unroll
      for (int t = 0; t < 5; ++t) ldsw128f(xb + t * 1024, acc[t]);
    }
    bar();
    if (K2 == 1) {
      const u32 ob = 102400 + T4 * 2816;
#pragma unroll
      for (int t = 0; t < 5; ++t) {
        const v4f a = acc[t] + ldsr128f(xb + t * 1024);
        const v4f gb = ldsr128f(L_WPHI + 320 + t * 64 + g4 * 16);
        ldsw64(ob + t16 * 176 + t * 32 + g4 * 8, v2u{pkbf(x1[4 * t] + gb[0] * a[0], x1[4 * t + 1] + gb[1] * a[1]), pkbf(x1[4 * t + 2] + gb[2] * a[2], x1[4 * t + 3] + gb[3] * a[3])});
      }
#pragma unroll
      for (int i = 0; i < 3; ++i) {
        const int it = i * 64 + lane, tk = it / 10, ch = it - tk * 10;
        if (it < 160) *(GAS v4u*)((GAS char*)out + (128 * P + 64 * TH + 16 * T4 + tk) * 1280 + CT * 160 + ch * 16) = ldsr128(ob + tk * 176 + ch * 16);
      }
    }
  }
  if (b == 0 && tid == 0) __hip_atomic_store((GAS u32*)(ws + WS_EPOCH), tag, RLX_AGENT);
  WST(15);
  STAMP(19);
}
}

extern "C" void kernel_launch(void* const* d_in, const int* in_sizes, int n_in, void* d_out, int out_size, void* d_ws, size_t ws_size, hipStream_t stream) {
  (void)in_sizes; (void)n_in; (void)out_size; (void)ws_size;
  (void)hipFuncSetAttribute((const void*)fused_kernel, hipFuncAttributeMaxDynamicSharedMemorySize, L_TOTAL);
  fused_kernel<<<256, 512, L_TOTAL, stream>>>((const u16*)d_in[0], (const u16*)d_in[1], (const u16*)d_in[2], (const u16*)d_in[3], (const u16*)d_in[5],
                                              (const u16*)d_in[6], (const u16*)d_in[7], (const u16*)d_in[8], (const u16*)d_in[9], (u16*)d_out, (char*)d_ws);
}
```

```cpp
#include <hip/hip_runtime.h>
#include <stdint.h>

typedef unsigned short u16;
typedef unsigned u32;
typedef unsigned long long u64;
typedef u32 v4u __attribute__((ext_vector_type(4)));
typedef u32 v2u __attribute__((ext_vector_type(2)));
typedef float v4f __attribute__((ext_vector_type(4)));
typedef float v2f __attribute__((ext_vector_type(2)));
typedef float v16f __attribute__((ext_vector_type(16)));
typedef short v4s __attribute__((ext_vector_type(4)));
typedef __bf16 v8bf __attribute__((ext_vector_type(8)));
typedef __bf16 v2bf __attribute__((ext_vector_type(2)));
typedef _Float16 v2h __attribute__((ext_vector_type(2)));
#define LAS __attribute__((address_space(3)))
#define GAS __attribute__((address_space(1)))
#define DI __device__ __forceinline__
#define RLX_AGENT __ATOMIC_RELAXED, __HIP_MEMORY_SCOPE_AGENT
#define SB() __builtin_amdgcn_sched_barrier(0)
typedef __amdgpu_buffer_rsrc_t rsrc_t;

#define ROT_ON 1
#define NTS " nt"
namespace {
constexpr int SEQ = 2048, DM = 640, FF = 1728;
constexpr u32 WS_EPOCH = 0;
constexpr u32 WS_FLAG = 1024;
constexpr u32 WS_ST1 = 61440;
constexpr u32 WS_MODG = WS_ST1 + 32768;
constexpr u32 WS_AB = WS_MODG + 4 * 30720;
constexpr u32 WS_KVP = WS_AB + 4 * 3456 * 16;
constexpr u32 KVP_STRIDE = 3456;
constexpr u32 WS_STAT = WS_KVP + 256 * KVP_STRIDE;
constexpr u32 WS_O = WS_STAT + SEQ * 64;
constexpr u32 WS_U = WS_O + SEQ * DM * 2;
constexpr u32 WS_ACT = WS_U + SEQ * DM * 2;
constexpr u32 WS_GU = WS_ACT + SEQ * FF * 2;
constexpr u32 WS_WD = WS_GU + 16 * 286720;
constexpr u32 WS_WQ = WS_WD + 8 * 276480;
constexpr u32 WS_WO = WS_WQ + 16 * 153600;
constexpr u32 WS_T = WS_WO + 16 * 102400;
constexpr u32 WS_END = WS_T + SEQ * DM * 2;
constexpr u32 WS_DBG = (WS_END + 4095) & ~4095u;
static_assert(WS_KVP % 128 == 0 && WS_STAT % 128 == 0 && WS_O % 256 == 0, "align");
constexpr u32 L_RQ = 102400;
constexpr u32 L_RAW = 102400;
constexpr u32 L_MISC = 153600;
constexpr u32 L_COA = L_MISC, L_COS = L_MISC + 2560;
constexpr u32 L_KVB = L_MISC + 5120;
constexpr u32 L_ZERO = L_KVB + 3360;
constexpr u32 L_WPHI = L_ZERO + 16;
constexpr u32 L_SYNC = L_WPHI + 1600;
constexpr u32 L_TOTAL = 163840;
constexpr u32 L_G2A = L_COA, L_G2S = L_COA + 640;
constexpr u32 L_G2R = L_COA + 1280;
constexpr u32 L_AB = L_COA + 3328;
static_assert(L_SYNC + 64 <= L_TOTAL && L_AB + 1792 <= L_KVB, "lds");
constexpr u32 L_WO = 32768;
constexpr int WO_EARLY = 48, KV_THR = 8, GU_THR = 6;
#define CODE_PF 49152
#define TLB_PF 1
#define CODE_BACK 0
constexpr u32 SPIN_CAP = 1u << 15;
constexpr int AUX_SC1 = 16;
constexpr u32 OOB = 0x7ffffff0u;

DI float bflo(u32 v) { return __uint_as_float(v << 16); }
DI float bfhi(u32 v) { return __uint_as_float(v & 0xffff0000u); }
DI u32 pkbf(float a, float b) { v2f t = {a, b}; return __builtin_bit_cast(u32, __builtin_convertvector(t, v2bf)); }
DI float rbf(float a) { return bflo(pkbf(a, a)); }
DI void bar() { asm volatile("s_waitcnt lgkmcnt(0)\n\ts_barrier" ::: "memory"); }
DI void rawbar() { asm volatile("s_barrier" ::: "memory"); }
DI void drain() { asm volatile("s_waitcnt vmcnt(0)" ::: "memory"); }
template <int N> DI void waitv() { asm volatile("s_waitcnt vmcnt(%0)" ::"i"(N) : "memory"); }
template <int I> struct IC { static constexpr int v = I; };
template <int B, int E, class F> DI void sfor(F&& f) { if constexpr (B < E) { f(IC<B>{}); sfor<B + 1, E>(f); } }
DI rsrc_t mkrs(const void* p, u32 bytes) { return __builtin_amdgcn_make_buffer_rsrc((void*)p, 0, bytes, 0x00020000); }
DI void dma16(rsrc_t rs, u32 voff, u32 soff, u32 lds) {
  asm volatile("s_mov_b32 m0, %0\n\ts_nop 0\n\tbuffer_load_dwordx4 %1, %2, %3 offen lds" ::"s"(lds), "v"(voff), "s"(rs), "s"(soff) : "memory");
}
template <int NL> DI void dmam(rsrc_t rs, u32 voff, u32 soff, u32 lds) {
  asm volatile("s_mov_b32 m0, %0\n\ts_mov_b32 exec_hi, %4\n\tbuffer_load_dwordx4 %1, %2, %3 offen" NTS " lds\n\ts_mov_b32 exec_hi, -1"
               ::"s"(lds), "v"(voff), "s"(rs), "s"(soff), "i"((1u << (NL - 32)) - 1u) : "memory");
}
DI void dma16c(rsrc_t rs, u32 voff, u32 soff, u32 lds) {
  asm volatile("s_mov_b32 m0, %0\n\ts_nop 0\n\tbuffer_load_dwordx4 %1, %2, %3 offen sc1 lds" ::"s"(lds), "v"(voff), "s"(rs), "s"(soff) : "memory");
}
template <int NL> DI void dmamc(rsrc_t rs, u32 voff, u32 soff, u32 lds) {
  asm volatile("s_mov_b32 m0, %0\n\ts_mov_b32 exec_hi, %4\n\tbuffer_load_dwordx4 %1, %2, %3 offen sc1 lds\n\ts_mov_b32 exec_hi, -1"
               ::"s"(lds), "v"(voff), "s"(rs), "s"(soff), "i"((1u << (NL - 32)) - 1u) : "memory");
}
DI u32 opq(u32 v) { asm volatile("" : "+v"(v)); return v; }
DI v4s trd(u32 addr) { return __builtin_amdgcn_ds_read_tr16_b64_v4i16((LAS v4s*)(size_t)addr); }
DI v8bf trfrag(u32 a0, u32 a1) {
  v4s a = trd(a0), b = trd(a1);
  return __builtin_bit_cast(v8bf, __builtin_shufflevector(a, b, 0, 1, 2, 3, 4, 5, 6, 7));
}
DI v16f mfma32(v8bf a, v8bf b, v16f c) { return __builtin_amdgcn_mfma_f32_32x32x16_bf16(a, b, c, 0, 0, 0); }
DI v4f mfma16(v8bf a, v8bf b, v4f c) { return __builtin_amdgcn_mfma_f32_16x16x32_bf16(a, b, c, 0, 0, 0); }
DI v8bf asbf(v4u v) { return __builtin_bit_cast(v8bf, v); }
DI v4u ldsr128(u32 a) { return *(LAS v4u*)(size_t)a; }
DI v4f ldsr128f(u32 a) { return *(LAS v4f*)(size_t)a; }
DI v2u ldsr64(u32 a) { return *(LAS v2u*)(size_t)a; }
DI void ldsw128(u32 a, v4u v) { *(LAS v4u*)(size_t)a = v; }
DI void ldsw128f(u32 a, v4f v) { *(LAS v4f*)(size_t)a = v; }
DI void ldsw64(u32 a, v2u v) { *(LAS v2u*)(size_t)a = v; }
DI float ldsrf(u32 a) { return *(LAS float*)(size_t)a; }
DI void ldswf(u32 a, float v) { *(LAS float*)(size_t)a = v; }
DI float mixlo(u32 t, float a, float s) { float r; asm("v_fma_mix_f32 %0, %1, %2, %3 op_sel_hi:[1,0,0]" : "=v"(r) : "v"(t), "v"(a), "v"(s)); return r; }
DI float mixhi(u32 t, float a, float s) { float r; asm("v_fma_mix_f32 %0, %1, %2, %3 op_sel:[1,0,0] op_sel_hi:[1,0,0]" : "=v"(r) : "v"(t), "v"(a), "v"(s)); return r; }
DI float xsum16(float v) {
  auto r = __builtin_amdgcn_permlane16_swap(__float_as_uint(v), __float_as_uint(v), false, false);
  return __uint_as_float(r[0]) + __uint_as_float(r[1]);
}
DI float xsum32(float v) {
  auto r = __builtin_amdgcn_permlane32_swap(__float_as_uint(v), __float_as_uint(v), false, false);
  return __uint_as_float(r[0]) + __uint_as_float(r[1]);
}
template <int CTRL> DI float dppadd(float v) {
  return v + __uint_as_float(__builtin_amdgcn_update_dpp(0u, __float_as_uint(v), CTRL, 0xf, 0xf, true));
}
constexpr int DPP_ROR4 = 0x124, DPP_ROR8 = 0x128;
DI float red8(const float (&a)[8], int lane) {
  float b4[4], c[2];
#pragma unroll
  for (int e = 0; e < 4; ++e) { auto r = __builtin_amdgcn_permlane32_swap(__float_as_uint(a[e]), __float_as_uint(a[4 + e]), false, false); b4[e] = __uint_as_float(r[0]) + __uint_as_float(r[1]); }
#pragma unroll
  for (int e = 0; e < 2; ++e) { auto r = __builtin_amdgcn_permlane16_swap(__float_as_uint(b4[e]), __float_as_uint(b4[2 + e]), false, false); c[e] = __uint_as_float(r[0]) + __uint_as_float(r[1]); }
  const float x = dppadd<DPP_ROR8>(c[0]), y = dppadd<DPP_ROR8>(c[1]);
  return (lane & 8) ? y : x;
}
DI v4u ldb128(rsrc_t rs, u32 vo, u32 so) { return __builtin_amdgcn_raw_buffer_load_b128(rs, vo, so, 2); }
DI v4u ldb128c(rsrc_t rs, u32 vo, u32 so) { return __builtin_amdgcn_raw_buffer_load_b128(rs, vo, so, AUX_SC1); }
template <int OFF> DI void aload(v4u& r, rsrc_t rs, u32 vo, u32 so) {
  asm volatile("buffer_load_dwordx4 %0, %1, %2, %3 offen offset:%4" NTS : "=v"(r) : "v"(vo), "s"(rs), "s"(so), "i"(OFF) : "memory");
}
template <int OFF> DI void aloadc(v4u& r, rsrc_t rs, u32 vo, u32 so) {
  asm volatile("buffer_load_dwordx4 %0, %1, %2, %3 offen offset:%4 sc1" : "=v"(r) : "v"(vo), "s"(rs), "s"(so), "i"(OFF) : "memory");
}
template <int N> DI void await1(v4u& a) { asm volatile("s_waitcnt vmcnt(%1)" : "+v"(a) : "i"(N) : "memory"); }
template <int N> DI void await2(v4u& a, v4u& b) { asm volatile("s_waitcnt vmcnt(%2)" : "+v"(a), "+v"(b) : "i"(N) : "memory"); }
template <int N> DI void await4(v4u& a, v4u& b, v4u& c, v4u& d) { asm volatile("s_waitcnt vmcnt(%4)" : "+v"(a), "+v"(b), "+v"(c), "+v"(d) : "i"(N) : "memory"); }
DI void astorec(v4u v, rsrc_t rs, u32 vo, u32 so) { u32 o = vo + so; asm volatile("" : "+v"(o)); __builtin_amdgcn_raw_buffer_store_b128(v, rs, o, 0, AUX_SC1); }
DI int sopq(int v) { asm volatile("" : "+s"(v)); return v; }
template <int N> DI void await3(v4u& a, v4u& b, v4u& c) { asm volatile("s_waitcnt vmcnt(%3)" : "+v"(a), "+v"(b), "+v"(c) : "i"(N) : "memory"); }
DI void stb128c(rsrc_t rs, u32 vo, v4u v) { __builtin_amdgcn_raw_buffer_store_b128(v, rs, vo, 0, AUX_SC1); }
DI void stb64c(rsrc_t rs, u32 vo, v2u v) { __builtin_amdgcn_raw_buffer_store_b64(v, rs, vo, 0, AUX_SC1); }

#define WST(i) do {} while (0)
#define STAMP(i) do {} while (0)

DI void setflag(char* ws, u32 off, u32 tag) { (void)__hip_atomic_exchange((GAS u32*)(ws + off), tag, RLX_AGENT); }
DI void bflag(rsrc_t rs, u32 tag, u32 off) { asm volatile("s_mov_b64 exec, 1\n\tbuffer_atomic_swap %0, off, %2, %1\n\ts_mov_b64 exec, -1" ::"v"(tag), "s"(off), "s"(rs) : "memory"); }
DI void publish(char* ws, u32 flagoff, u32 tag) {
  drain();
  bar();
  if (threadIdx.x == 0) setflag(ws, flagoff, tag);
}
DI void poll_n(char* ws, u32 off, int n, u32 tag, int lane) {
  GAS u32* f = (GAS u32*)(ws + off) + lane;
#pragma unroll 1
  for (u32 spin = 0; spin < SPIN_CAP; ++spin) {
    u32 v = tag;
    if (lane < n) { v = 0; asm volatile("global_atomic_or %0, %1, %0, off sc0 sc1\n\ts_waitcnt vmcnt(0)" : "+v"(v) : "v"(f) : "memory"); }
    if (__all(v == tag)) break;
    __builtin_amdgcn_s_sleep(1);
  }
}
DI void lds_post(u32 a, u32 tag) { asm volatile("" ::: "memory"); *(volatile LAS u32*)(size_t)a = tag; }
DI void lds_wait(u32 a, u32 tag) {
#pragma unroll 1
  for (u32 spin = 0; spin < (SPIN_CAP << 4); ++spin) {
    if (__all(*(volatile LAS u32*)(size_t)a == tag)) break;
    __builtin_amdgcn_s_sleep(2);
  }
  asm volatile("" ::: "memory");
}
DI void gather2(rsrc_t rs, u32 o1, u32 o2, u32 tag, v4u& g1, v4u& g2) {
#pragma unroll 1
  for (u32 spin = 0; spin < SPIN_CAP; ++spin) {
    asm volatile("" ::: "memory");
    g1 = ldb128c(rs, o1, 0);
    g2 = ldb128c(rs, o2, 0);
    if (__all(g1[1] == tag && g1[3] == tag && g2[1] == tag && g2[3] == tag)) break;
    __builtin_amdgcn_s_sleep(1);
  }
}
DI void gather4(rsrc_t rs, u32 o, u32 st, u32 tag, v4u& g0, v4u& g1, v4u& g2, v4u& g3) {
#pragma unroll 1
  for (u32 spin = 0; spin < SPIN_CAP; ++spin) {
    asm volatile("" ::: "memory");
    g0 = ldb128c(rs, o, 0); g1 = ldb128c(rs, o, st); g2 = ldb128c(rs, o, 2 * st); g3 = ldb128c(rs, o, 3 * st);
    if (__all(g0[1] == tag && g0[3] == tag && g1[1] == tag && g1[3] == tag && g2[1] == tag && g2[3] == tag && g3[1] == tag && g3[3] == tag)) break;
    __builtin_amdgcn_s_sleep(1);
  }
}
DI v2f sum4(const v4u& a, const v4u& b, const v4u& c, const v4u& d, float one) {
  const float x = (__uint_as_float(a[0]) + __uint_as_float(b[0])) + (__uint_as_float(c[0]) + __uint_as_float(d[0]));
  const float y = (__uint_as_float(a[2]) + __uint_as_float(b[2])) + (__uint_as_float(c[2]) + __uint_as_float(d[2]));
  return v2f{rbf(one + rbf(x)), rbf(one + rbf(y))};
}
DI bool tags4(const v4u& a, const v4u& b, const v4u& c, const v4u& d, u32 t) {
  return __all(a[1] == t && a[3] == t && b[1] == t && b[3] == t && c[1] == t && c[3] == t && d[1] == t && d[3] == t);
}
DI v4f unp4(v4u g, float one) { return v4f{rbf(one + bflo(g[0])), rbf(one + bfhi(g[0])), rbf(one + bflo(g[2])), rbf(one + bfhi(g[2]))}; }

DI void softmax20(const float (&p)[20], u32 (&o)[10]) {
  float mx = p[0];
#pragma unroll
  for (int i = 1; i < 20; ++i) mx = fmaxf(mx, p[i]);
  const float L2E = 1.4426950408889634f, a = -mx * L2E;
  float e[20], s0 = 0.f, s1 = 0.f;
#pragma unroll
  for (int i = 0; i < 20; ++i) { e[i] = __builtin_amdgcn_exp2f(__builtin_fmaf(p[i], L2E, a)); if (i & 1) s1 += e[i]; else s0 += e[i]; }
  const float r = __builtin_amdgcn_rcpf(s0 + s1);
#pragma unroll
  for (int i = 0; i < 10; ++i) o[i] = pkbf(e[2 * i] * r, e[2 * i + 1] * r);
}
__global__ __launch_bounds__(512, 2) void fused_kernel(const u16* __restrict__ x, const u16* __restrict__ wqkv, const u16* __restrict__ wphi,
                                                       const u16* __restrict__ wo, const u16* __restrict__ wg, const u16* __restrict__ wu,
                                                       const u16* __restrict__ wd, const u16* __restrict__ cvec, const u16* __restrict__ wmod,
                                                       u16* __restrict__ out, char* ws) {
  const int tid = threadIdx.x, lane = tid & 63;
  const int wave = __builtin_amdgcn_readfirstlane(tid >> 6);
  const int b = blockIdx.x, xcd = b & 7, idx = b >> 3;
  __builtin_assume(wave >= 0 && wave < 8);
  __builtin_assume(b >= 0 && b < 256);
  auto touch = [&](u64 SL, u64 OF, int e) {
    const u32 sl = (u32)(SL >> (e * 4)) & 15u, of = (u32)(OF >> (e * 4)) & 15u;
    const char* base = ((const char* const __attribute__((address_space(4)))*)__builtin_amdgcn_kernarg_segment_ptr())[sl];
    const rsrc_t rs_t = mkrs(base, 0x7fffffff);
    asm volatile("s_mov_b32 m0, %3\n\ts_mov_b64 exec, 1\n\tbuffer_load_dword %0, %1, %2 offen lds\n\ts_mov_b64 exec, -1" ::"v"(0), "s"(rs_t), "s"(of << 20), "i"(L_KVB + 2048) : "memory");
  };
  if (wave == 0) touch(0x72111000a8888a78ull, 0x0021021004321000ull, idx & 15);
  SB();
  {
    const rsrc_t rs_cd = mkrs((const void*)(__builtin_amdgcn_s_getpc() & ~(u64)127), CODE_PF);
    asm volatile("s_mov_b32 m0, 0\n\ts_mov_b64 exec, 3\n\tbuffer_load_dword %0, %1, %2 offen lds\n\ts_mov_b64 exec, -1" ::"v"(lane * 32768), "s"(rs_cd), "s"((wave * 32 + idx) * 128) : "memory");
  }
  SB();
  STAMP(0);
  const int T4 = wave & 3, K2 = wave >> 2;
  const int mc = b >> 2, mk = b & 3;
  const u32 coff2 = (160 * mk + 40 * T4 + 5 * (lane >> 3)) * 2;
  const int P = 4 * (xcd >> 1) + (idx >> 3);
  const int J = 8 * (xcd & 1) + (idx & 7);
  const int TH = J >> 3, CT = J & 7;
  const rsrc_t rs_ws = mkrs(ws, WS_END);
  const rsrc_t rs_x = mkrs(x, SEQ * DM * 2);
  const u32 vlin = lane * 16;
  const int hh = lane >> 5, i32 = lane & 31, g4 = lane >> 4, t16 = lane & 15;
  const int g2 = g4 & 1, q4 = t16 >> 2, p4 = lane & 3;
  v4u wm[5], cq, tagq;
  {
    const rsrc_t rs_a = mkrs(K2 ? x : wmod, K2 ? SEQ * DM * 2 : DM * 6 * DM * 2), rs_c = mkrs(cvec, DM * 2);
    const u32 vo = K2 ? (128 * P + 8 * J + 2 * T4 + hh) * 1280 + i32 * 16 : coff2 * 3840 + mc * 128 + (lane & 7) * 16;
    aload<0>(cq, rs_c, coff2 & ~3u, 0);
    sfor<0, 5>([&](auto I) { aload<0>(wm[I.v], rs_a, vo, K2 ? (I.v < 3 ? I.v * 512 : 0x10000000) : I.v * 7680); });
    aloadc<0>(tagq, rs_ws, WS_EPOCH, 0);
  }
  SB();
  ldsw128(L_SYNC + (lane & 3) * 16, v4u{0u, 0u, 0u, 0u});
  ldsw128(L_ZERO, v4u{0u, 0u, 0u, 0u});
  const int rot32 = (idx & 7) * 4 + (idx >> 3);
  int R20 = (rot32 * 20) >> 5;
  const int R4A = rot32 >> 1, R4B = (rot32 * 11) >> 5;
  auto wrap = [](int v, int m) { return v >= m ? v - m : v; };
  const int t0 = 7 * J - (J >> 2), nt = (J & 3) == 3 ? 6 : 7;
  const int rq27 = 2 * P + TH;
  const u32 wqs = WS_WQ + J * 153600;
  auto tile = [&](u32 src, u32 rstride, u32 dst, u32 so) {
    astorec(__builtin_bit_cast(v4u, trfrag(src + q4 * rstride, src + (4 + q4) * rstride)), rs_ws, dst, so);
  };
  const int l10 = lane / 10, c10 = lane - l10 * 10;
  const u32 v10 = l10 * 1280 + c10 * 16;
  constexpr u32 L_R2 = L_RAW + 11520;
  constexpr u32 L_RD = L_R2 + 17920;
  auto rawloads = [&]() {
    const rsrc_t rs_d = mkrs(wd, FF * DM * 2);
    const int row = lane / 14; int ch = lane - row * 14; if (ch >= 2 * nt) ch -= 2;
    const u32 vg = row * 3456 + ch * 16;
#pragma unroll 1
    for (int m = 0; m < 2; ++m) {
      const rsrc_t rs_g = mkrs(m ? wu : wg, DM * FF * 2);
#pragma unroll 1
      for (int i = 0; i < 10; ++i) dmam<56>(rs_g, vg, (40 * P + 4 * i) * 3456 + t0 * 32, L_R2 + m * 8960 + i * 896);
    }
    if (rq27 < 27) {
#pragma unroll 1
      for (int z = 0; z < 4; ++z) {
        const int o = 8 * rq27 + 2 * z;
        int jj, oo;
        if (o < 128) { jj = o >> 3; oo = o & 7; } else if (o < 192) { jj = (o - 128) >> 2; oo = 8 + (o & 3); } else { const int hq = (o - 192) >> 1; jj = hq + hq / 3; oo = 12; }
        const int wdrow = (7 * jj - (jj >> 2)) * 16 + oo * 8;
#pragma unroll 1
        for (int i = 0; i < 4; ++i) dmam<40>(rs_d, v10, (wdrow + 4 * i) * 1280 + CT * 160, L_RD + z * 2560 + i * 640);
      }
    }
  };
  constexpr u32 L_WR = 81920, L_OR = 92160;
  u32 ta[2] = {}, td[2][2] = {};
  if (K2) {
    {
      const rsrc_t rs_w = mkrs(wqkv, DM * 3 * DM * 2), rs_2 = mkrs(T4 == 1 ? wphi : wqkv, T4 == 1 ? 32 * 400 * 2 : DM * 3 * DM * 2);
      const int r = lane / 15, c = lane - r * 15, pt = c / 5;
      const u32 vo = r * 3840 + pt * 1280 + (c - pt * 5) * 16, so = (40 * P + 8 * T4) * 3840 + J * 80;
      dmam<60>(rs_w, vo, so, L_WR + T4 * 1920); dmam<60>(rs_w, vo, so + 4 * 3840, L_WR + T4 * 1920 + 960);
      const u32 v2 = T4 == 1 ? vlin : vo, s2 = T4 == 1 ? J * 1600 : (40 * P + 32) * 3840 + J * 80;
      dmam<60>(rs_2, v2, s2, T4 == 1 ? L_WPHI : L_WR + 7680); dmam<60>(rs_2, v2, s2 + (T4 == 1 ? 640 : 4 * 3840), T4 == 1 ? L_WPHI + 640 : L_WR + 8640);
    }
  {
    const u32 cm = (p4 >> 1) * 40 + (p4 & 1) * 16;
    ta[0] = opq(L_WR + q4 * 240 + 80 + hh * 80 + cm + g2 * 8);
    ta[1] = opq(L_WR + q4 * 240 + (g4 == 0 ? 80 + (p4 & 1) * 80 + (p4 >> 1) * 40 + 32 : g4 == 3 ? (p4 & 1) * 40 + 32 : cm + (g4 - 1) * 8));
    const u32 sA = g4 == 0 ? 5120 : 2560, sB = g4 == 3 ? 128 : 256, sC = (g4 == 0 ? 4096 : (g4 == 3 && t16 >= 8) ? OOB : 102400 + (g4 - 1) * 1024) + t16 * 16;
#pragma unroll
    for (int z = 0; z < 2; ++z) {
      const int oct = 5 * P + (z ? 4 : T4);
      td[z][0] = opq(wqs + (oct >> 2) * 5120 + (oct & 3) * 256 + g4 * 1024 + t16 * 16);
      td[z][1] = opq(wqs + (oct >> 2) * sA + (oct & 3) * sB + sC);
    }
  }
  }
  u32 tag;
  bar();
  await1<6>(cq);
  float sv[5];
  {
    const u32 sh = (coff2 & 2) * 8;
    const u32 ce[3] = {__builtin_amdgcn_alignbit(cq[1], cq[0], sh), __builtin_amdgcn_alignbit(cq[2], cq[1], sh), cq[2] >> sh};
#pragma unroll
    for (int i = 0; i < 5; ++i) {
      const float cv = (i & 1) ? bfhi(ce[i >> 1]) : bflo(ce[i >> 1]);
      sv[i] = rbf(cv * __builtin_amdgcn_rcpf(1.f + __builtin_amdgcn_exp2f(-1.4426950408889634f * cv)));
      asm volatile("" : "+v"(sv[i]));
    }
  }
  await1<0>(tagq);
  asm volatile("" : "+v"(wm[0]), "+v"(wm[1]), "+v"(wm[2]), "+v"(wm[3]), "+v"(wm[4]));
  tag = tagq[0] + 1u;
  WST(0);
  if (!K2) {
    float acc[8] = {0.f, 0.f, 0.f, 0.f, 0.f, 0.f, 0.f, 0.f};
#pragma unroll
    for (int i = 0; i < 5; ++i)
#pragma unroll
      for (int e = 0; e < 4; ++e) { acc[2 * e] += sv[i] * bflo(wm[i][e]); acc[2 * e + 1] += sv[i] * bfhi(wm[i][e]); }
    ldswf(L_KVB + T4 * 256 + ((lane & 7) * 8 + 4 * hh + 2 * g2 + ((lane >> 3) & 1)) * 4, red8(acc, lane));
    if (wave == 1) touch(0x9993339993339993ull, 0x2100002100002100ull, idx & 15);
    if (wave) lds_post(L_SYNC + 12 + wave * 4, 1u);
    else {
      lds_wait(L_SYNC + 16 + (lane % 3) * 4, 1u);
      const u32 a = L_KVB + lane * 4;
      const float v = (ldsrf(a) + ldsrf(a + 256)) + (ldsrf(a + 512) + ldsrf(a + 768));
      stb64c(rs_ws, b < 240 ? WS_MODG + (mk * 3840 + mc * 64 + lane) * 8 : OOB, v2u{__float_as_uint(v), tag});
      drain();
      const int vec = mc >= 10;
      if (mc < 20 && lane < 32 && ((40 * t16 + 39 * g2) >> 6) == mc - 10 * vec) setflag(ws, WS_FLAG + (6 * 16 + t16) * 512 + 256 + (vec * 8 + g2 * 4 + mk) * 4, tag);
    }
    WST(1);
    {
      const int r5 = lane / 5;
      const u32 vs = r5 * 1280 + (lane - r5 * 5) * 16, so = (128 * P + 32 * T4) * 1280 + J * 80, lx = L_RAW + T4 * 2880;
      dmam<60>(rs_x, vs, so, lx); dmam<60>(rs_x, vs, so + 12 * 1280, lx + 960); dmam<40>(rs_x, vs, so + 24 * 1280, lx + 1920);
    }
    if (wave == 1) rawloads();
    if (wave == 2) {
      const int e = lane < 40 ? lane : 39, vec = e >= 20;
      v4u g0, g1, g2v, g3;
      poll_n(ws, WS_FLAG + (6 * 16 + J) * 512 + 256, 16, tag, lane);
      gather4(rs_ws, WS_MODG + (vec * 640 + 40 * J) * 8 + (e - 20 * vec) * 16, 30720, tag, g0, g1, g2v, g3);
      *(LAS v2f*)(size_t)(L_COA + e * 8) = sum4(g0, g1, g2v, g3, vec ? 1.f : 0.f);
      lds_post(L_SYNC, 1u);
    } else if (wave == 3) {
      v4u g0, g1;
      poll_n(ws, WS_FLAG + (5 * 16 + P) * 512 + 256, 64, tag, lane);
      gather2(rs_ws, WS_ST1 + P * 2048 + vlin, WS_ST1 + P * 2048 + 1024 + vlin, tag, g0, g1);
      ldsw64(L_COS + lane * 8, v2u{g0[0], g0[2]}); ldsw64(L_COS + 512 + lane * 8, v2u{g1[0], g1[2]});
      lds_post(L_SYNC + 4, 1u);
    }
    drain();
    v4u xs[3];
    xs[0] = ldsr128(L_RAW + T4 * 2880 + t16 * 80 + g4 * 16); xs[1] = ldsr128(L_RAW + T4 * 2880 + 1280 + t16 * 80 + g4 * 16); xs[2] = ldsr128(L_RAW + T4 * 2880 + i32 * 80 + 64);
    WST(4);
    lds_wait(L_SYNC + (lane & 1) * 4, 1u);
    const u32 cw = L_COA;
    const v2u s0 = ldsr64(L_COS + (32 * T4 + t16) * 8), s1 = ldsr64(L_COS + (32 * T4 + 16 + t16) * 8), s2 = g2 ? s1 : s0;
    auto hch = [&](const v4u& xv, u32 cb, u32 mu, u32 ru) {
      const float rs = __uint_as_float(ru), mr = -__uint_as_float(mu) * rs;
      v4u o;
#pragma unroll
      for (int z = 0; z < 2; ++z) {
        const v4f S = ldsr128f(cb + z * 16), A = ldsr128f(cb + 160 + z * 16);
        o[2 * z] = pkbf(__builtin_fmaf(__builtin_fmaf(bflo(xv[2 * z]), rs, mr), A[0], S[0]), __builtin_fmaf(__builtin_fmaf(bfhi(xv[2 * z]), rs, mr), A[1], S[1]));
        o[2 * z + 1] = pkbf(__builtin_fmaf(__builtin_fmaf(bflo(xv[2 * z + 1]), rs, mr), A[2], S[2]), __builtin_fmaf(__builtin_fmaf(bfhi(xv[2 * z + 1]), rs, mr), A[3], S[3]));
      }
      return o;
    };
    const u32 tb = WS_T + ((8 * P + 2 * T4) * 80 + 5 * J) * 256;
    stb128c(rs_ws, tb + vlin, hch(xs[0], cw + g4 * 32, s0[0], s0[1]));
    stb128c(rs_ws, tb + 20480 + vlin, hch(xs[1], cw + g4 * 32, s1[0], s1[1]));
    stb128c(rs_ws, lane < 32 ? tb + g2 * 20480 + 1024 + t16 * 16 : OOB, hch(xs[2], cw + 128, s2[0], s2[1]));
    drain();
    if (lane == 0) setflag(ws, WS_FLAG + (6 * 16 + P) * 512 + (T4 * 16 + J) * 4, tag);
    WST(3);
    STAMP(1);
  } else {
    {
      const u32 ro = T4 * 1920;
      const v8bf f0 = trfrag(ta[0] + ro, ta[0] + ro + 960), f1 = trfrag(ta[1] + ro, ta[1] + ro + 960);
      const u32 tz = (T4 >> 1) ? ta[T4 & 1] : ta[0];
      const v8bf f2 = trfrag(tz + 7680, tz + 8640);
      stb128c(rs_ws, td[0][0], __builtin_bit_cast(v4u, f0)); stb128c(rs_ws, td[0][1], __builtin_bit_cast(v4u, f1));
      stb128c(rs_ws, (T4 >> 1) ? ((T4 & 1) ? td[1][1] : td[1][0]) : OOB, __builtin_bit_cast(v4u, f2));
    }
    {
      float tn[24];
#pragma unroll
      for (int z = 0; z < 3; ++z)
#pragma unroll
        for (int e = 0; e < 4; ++e) { tn[8 * z + 2 * e] = bflo(wm[z][e]); tn[8 * z + 2 * e + 1] = bfhi(wm[z][e]); }
      auto hsum = [](float a) { return xsum16(dppadd<DPP_ROR8>(dppadd<DPP_ROR4>(dppadd<0x4e>(dppadd<0xb1>(a))))); };
      float sm = 0.f, sm2 = 0.f;
#pragma unroll
      for (int e = 0; e < 24; ++e) { if (e < 16) sm += tn[e]; else sm2 += tn[e]; }
      const float m = hsum(i32 < 16 ? sm + sm2 : sm) * (1.f / DM);
      float q = 0.f, q2 = 0.f;
#pragma unroll
      for (int e = 0; e < 24; ++e) { const float d = tn[e] - m; if (e < 16) q += d * d; else q2 += d * d; }
      const float rs = __builtin_amdgcn_rsqf(hsum(i32 < 16 ? q + q2 : q) * (1.f / DM) + 1e-5f);
      stb64c(rs_ws, i32 < 2 ? WS_ST1 + (128 * P + 8 * J + 2 * T4 + hh) * 16 + i32 * 8 : OOB, v2u{__float_as_uint(i32 ? rs : m), tag});
    }
    drain();
    if (lane < 2) setflag(ws, WS_FLAG + (lane ? (5 * 16 + J) * 512 + (P * 4 + T4) * 4 : (5 * 16 + P) * 512 + 256 + (J * 4 + T4) * 4), tag);
    WST(1);
    if (wave == 4) { poll_n(ws, WS_FLAG + (5 * 16 + J) * 512, 64, tag, lane); lds_post(L_SYNC + 8, 1u); }
    else {
      const rsrc_t rs_wo = mkrs(wo, DM * DM * 2);
      const int o0 = 2 * T4 - 2;
      const u32 so = (40 * P + 8 * o0) * 1280 + CT * 160, lr = L_OR + o0 * 1280;
      dmam<40>(rs_wo, v10, so, lr); dmam<40>(rs_wo, v10, so + 4 * 1280, lr + 640);
      if (T4 < 3) { dmam<40>(rs_wo, v10, so + 8 * 1280, lr + 1280); dmam<40>(rs_wo, v10, so + 12 * 1280, lr + 1920); }
      drain();
#pragma unroll 1
      for (int z = 0; z < 3; ++z) {
        const int ul = 4 * z + g4, hi = ul >= 5, tl = ul - 5 * hi, oct = 5 * P + o0 + hi;
        const bool ok = ul < (T4 < 3 ? 10 : 5);
        tile(lr + (ok ? hi * 1280 + tl * 32 : 0) + p4 * 8, 160, ok ? (oct >> 2) * 5120 + tl * 1024 + (oct & 3) * 256 + t16 * 16 : OOB, WS_WO + J * 102400);
      }
      lds_post(L_SYNC + 32 + wave * 4, 1u);
      lds_wait(L_SYNC + 8, 1u);
    }
    WST(3);
#pragma unroll 1
    for (int e = T4; e < 100; e += 4) {
      if (e >= 88 && e < 92) lds_wait(L_SYNC + 52 + (lane % 3) * 4, 1u);
      dma16c(rs_ws, vlin, wqs + wrap(e + 5 * R20, 100) * 1024, e * 1024);
    }
    WST(4);
  }
  WST(5);
  STAMP(2);
  u32 ts0[4], ts1[4], tdd[4];
#pragma unroll
  for (int z = 0; z < 4; ++z) {
    const int k = wave + 8 * z;
    u32 src, rst, dst;
    if (k < 18) {
      const bool blk = k < 15;
      const int o5b = k / 3, bk = k - o5b * 3, o5 = blk ? o5b : 2 * (k - 15) + hh, oct = 5 * P + o5;
      src = L_R2 + (o5 < 5 ? o5 : 0) * 1792 + (blk ? hh * 8960 + bk * 64 + p4 * 16 + g2 * 8 : g2 * 8960 + 192 + p4 * 8); rst = 224;
      dst = WS_GU + J * 286720 + (oct & 3) * 256 + t16 * 16 + (blk ? (bk < 2 ? (oct >> 2) * 8192 + bk * 4096 : 163840 + (oct >> 2) * 6144) + g4 * 1024 : 163840 + (oct >> 2) * 6144 + 4096 + g2 * 1024);
      dst = o5 < 5 ? dst : OOB;
    } else {
      const int kk = k < 28 ? k - 18 : 0, og = kk >= 5, tl = kk - 5 * og;
      src = L_RD + (4 * og + g4) * 1280 + tl * 32 + p4 * 8; rst = 160;
      dst = (k < 28 && rq27 < 27) ? WS_WD + CT * 276480 + (2 * rq27 + og) * 5120 + tl * 1024 + vlin : OOB;
    }
    ts0[z] = opq(src + q4 * rst); ts1[z] = opq(src + (4 + q4) * rst); tdd[z] = opq(dst);
  }
  if (wave == 0) poll_n(ws, WS_FLAG + (6 * 16 + P) * 512, 64, tag, lane);
  drain();
  bar();
  WST(6);
  STAMP(3);
  v4u hf[20];
  u32 lb[3];
  lb[0] = opq(vlin); lb[1] = opq(vlin + 51200); lb[2] = opq(vlin + 102400);
  auto frag = [&](u32 off) { return asbf(ldsr128(lb[off / 51200] + off % 51200)); };
  v4f aC[5] = {};
  v4u wpf[5][2];
  {
    constexpr int H0 = 8;
    const u32 hvo = WS_T + (8 * P + wave) * 20480 + vlin;
    auto ldh = [&](auto N) { aloadc<0>(hf[N.v], rs_ws, hvo, wrap(N.v + R20, 20) * 1024); };
    sfor<0, H0>(ldh);
  {
    const u32 mk0 = (q4 >> 1) == hh ? 0xffffffffu : 0u, mk1 = g2 ? 0u : mk0, sgn = (q4 & 1) ? 0x80008000u : 0u;
    const u32 r0 = L_WPHI + hh * 800 + q4 * 40;
#pragma unroll
    for (int T = 0; T < 5; ++T) {
      const u32 c = ((p4 & 1) ? (T + 1) % 5 : T) * 8;
      wpf[T][0] = __builtin_bit_cast(v4u, trfrag(r0 + g2 * 320 + c, r0 + g2 * 320 + 160 + c));
      const v2u l = __builtin_bit_cast(v2u, trd(r0 + 640 + c));
#pragma unroll
      for (int e = 0; e < 4; ++e) wpf[T][0][e] = (wpf[T][0][e] ^ sgn) & mk0;
      wpf[T][1] = v4u{(l[0] ^ sgn) & mk1, (l[1] ^ sgn) & mk1, 0u, 0u};
      asm volatile("" : "+v"(wpf[T][0]), "+v"(wpf[T][1]));
    }
  }
    v8bf wf[10], tfr;
    sfor<0, 10>([&](auto F) { wf[F.v] = frag(F.v * 1024); });
    SB();
    sfor<0, 20>([&](auto N) {
      constexpr int n = N.v;
      constexpr auto nst = [](int a, int b2) { int c = 0; for (int m = a < 0 ? 0 : a; m < b2; ++m) c += (m >= 2 && m < 10 && (m & 1) == 1) ? 1 : 0; return c; };
      if constexpr (n == 12) { rawbar(); SB(); }
      await1<7 + nst(n - 8, n)>(hf[n]);
      sfor<0, 5>([&](auto T) {
        constexpr int t = T.v, f = (5 * n + t) % 10;
        aC[t] = mfma16(wf[f], asbf(hf[n]), aC[t]);
        if constexpr (n < 18) wf[f] = frag((5 * n + 10 + t) * 1024);
        if constexpr (t == 0 && n < 12) ldh(IC<(n < 12 ? n + H0 : 0)>{});
        if constexpr (t == 2 && n >= 2 && n < 10) {
          constexpr int z = (n - 2) >> 1;
          if constexpr ((n & 1) == 0) tfr = trfrag(ts0[z], ts1[z]); else stb128c(rs_ws, tdd[z], __builtin_bit_cast(v4u, tfr));
        }
        if constexpr (t == 0 && n >= 12 && n < 19) {
          const int e = n < 18 ? 8 * (n - 12) + wave : 48 + (wave & 1);
          dma16c(rs_ws, vlin, wqs + 102400 + wrap(e + 5 * (R20 >> 1), 50) * 1024, L_RQ + e * 1024);
        }
        SB();
      });
    });
    asm volatile("" : "+v"(aC[0]), "+v"(aC[1]), "+v"(aC[2]), "+v"(aC[3]), "+v"(aC[4]));
  }
  WST(7);
  STAMP(4);
  auto phi = [&](const v4f& m0, const v4f& m1, const v4f& l, u32 (&o)[10]) {
    const v4u b0 = {pkbf(m0[0], m0[1]), pkbf(m0[2], m0[3]), pkbf(m1[0], m1[1]), pkbf(m1[2], m1[3])};
    const v4u b1 = {pkbf(l[0], l[1]), pkbf(l[2], l[3]), 0u, 0u};
    float pv[20];
#pragma unroll
    for (int T = 0; T < 5; ++T) {
      v4f d = {};
      d = mfma16(asbf(wpf[T][0]), asbf(b0), d); d = mfma16(asbf(wpf[T][1]), asbf(b1), d);
#pragma unroll
      for (int e = 0; e < 4; ++e) pv[4 * T + e] = d[e];
    }
    softmax20(pv, o);
  };
  {
    u32 pk[10];
    phi(aC[0], aC[1], aC[4], pk);
    const int tok = 16 * wave + t16;
    const u32 fx = (((tok >> 1) & 1) << 2) | ((tok & 1) << 1) | ((tok >> 2) & 1), rb = hh * 16384 + tok * 128;
    auto cad = [&](int cc) { return rb + ((cc ^ fx) << 4); };
    bar();
    ldsw128(cad(g2), v4u{pkbf(aC[2][0], aC[2][1]), pkbf(aC[2][2], aC[2][3]), pkbf(aC[3][0], aC[3][1]), pkbf(aC[3][2], aC[3][3])});
    if (g2) ldsw128(cad(2), v4u{pkbf(aC[4][0], aC[4][1]), pkbf(aC[4][2], aC[4][3]), 0x00003f80u, 0u});
    ldsw128(cad(g2 ? 7 : 4), v4u{pk[0], pk[1], pk[2], pk[3]});
    ldsw128(cad(g2 ? 3 : 5), v4u{pk[4], pk[5], pk[6], pk[7]});
    ldsw64(cad(6) + g2 * 8, v2u{pk[8], pk[9]});
  }
  drain();
  bar();
  STAMP(5);
  if (wave < 4) {
    const int hd = wave >> 1, tl = wave & 1;
    u32 av[2], ap[2];
#pragma unroll
    for (int t = 0; t < 2; ++t) {
      const int tok = 8 * hh + 4 * t + q4;
      const u32 fx = (((tok >> 1) & 1) << 2) | ((tok & 1) << 1) | ((tok >> 2) & 1), cl = 2 * g2 + (p4 >> 1);
      av[t] = hd * 16384 + tok * 128 + ((cl ^ fx) << 4) + 8 * (p4 & 1);
      ap[t] = hd * 16384 + tok * 128 + ((((tl ? 0 : 4) + cl) ^ fx) << 4) + 8 * (p4 & 1);
    }
    v8bf fv[8], fp[8];
#pragma unroll
    for (int s = 0; s < 8; ++s) { fp[s] = trfrag(ap[0] + s * 2048, ap[1] + s * 2048); fv[s] = trfrag(av[0] + s * 2048, av[1] + s * 2048); }
    v16f d = {};
#pragma unroll
    for (int s = 0; s < 8; ++s) d = mfma32(fp[s], fv[s], d);
    const u32 base = i32 < 21 ? WS_KVP + (J * 16 + P) * KVP_STRIDE + hd * 1680 + i32 * 8 + hh * 168 : OOB;
    if (tl == 0) {
#pragma unroll
      for (int rq = 0; rq < 3; ++rq) stb64c(rs_ws, base + rq * 336, v2u{pkbf(d[4 * rq], d[4 * rq + 1]), pkbf(d[4 * rq + 2], d[4 * rq + 3])});
    }
    stb64c(rs_ws, base + (3 + tl) * 336, v2u{pkbf(d[12], d[13]), pkbf(d[14], d[15])});
    drain();
    if (lane == 0) setflag(ws, WS_FLAG + (0 * 16 + J) * 512 + (P * 4 + wave) * 4, tag);
    STAMP(6);
  }
  WST(8);
  auto wo1 = [&](int e) { dma16c(rs_ws, vlin, WS_WO + J * 102400 + e * 1024, e * 1024); };
  u32 pq[10];
  {
    v4f aQ[3] = {};
    const int odd = R20 & 1;
    const u32 bq = opq(L_RQ + odd * 2560 + vlin), bl = opq(L_RQ + odd * 2560 + 2048 + g4 * 128 + ((t16 >> 3) * 4 + p4) * 16);
    const u32 bq19 = bq + (odd ? -2560 : 19 * 2560), bl19 = bl + (odd ? -2560 : 19 * 2560);
    auto qf = [&](int n, int t) { return asbf(ldsr128(n == 19 ? (t == 2 ? bl19 : bq19 + t * 1024) : (t == 2 ? bl + n * 2560 : bq + n * 2560 + t * 1024))); };
    v8bf wf[9];
#pragma unroll
    for (int f = 0; f < 9; ++f) wf[f] = qf(f / 3, f % 3);
    SB();
#pragma unroll
    for (int n = 0; n < 60; ++n) {
      aQ[n % 3] = mfma16(wf[n % 9], asbf(hf[n / 3]), aQ[n % 3]);
      if (n + 9 < 60) wf[n % 9] = qf((n + 9) / 3, n % 3);
      SB();
    }
    asm volatile("" : "+v"(aQ[0]), "+v"(aQ[1]), "+v"(aQ[2]));
    phi(aQ[0], aQ[1], aQ[2], pq);
#pragma unroll
    for (int i = 0; i < 10; ++i) asm volatile("" : "+v"(pq[i]));
  }
  WST(9);
  const int bq = sopq(b);
  const bool dog2 = bq < 216;
  const int g2c = bq >> 2, g2k = bq & 3;
  if (wave < 4) {
    const rsrc_t rs_gu = mkrs(g2c < 27 ? wg : wu, DM * FF * 2);
    const u32 vo = dog2 ? (160 * g2k + 40 * T4 + (lane >> 3)) * 3456 + (g2c < 27 ? g2c : g2c - 27) * 128 + (lane & 7) * 16 : OOB;
    v4u gw[5], g0, g1, g2v, g3;
#pragma unroll
    for (int i = 0; i < 5; ++i) gw[i] = ldb128(rs_gu, vo, i * 8 * 3456);
    const int e = lane < 40 ? lane : 39, vec = e >= 20;
    gather4(rs_ws, WS_MODG + (1920 + vec * 640 + 160 * g2k + 40 * T4) * 8 + (e - 20 * vec) * 16, 30720, tag, g0, g1, g2v, g3);
    *(LAS v2f*)(size_t)(L_G2A + T4 * 320 + e * 8) = sum4(g0, g1, g2v, g3, vec ? 1.f : 0.f);
    float sa[8] = {}, sb2[8] = {};
#pragma unroll
    for (int i = 0; i < 5; ++i) {
      const float cs = ldsrf(L_G2A + T4 * 320 + (8 * i + (lane >> 3)) * 4), ca = ldsrf(L_G2A + T4 * 320 + 160 + (8 * i + (lane >> 3)) * 4);
#pragma unroll
      for (int z = 0; z < 4; ++z) {
        const float lo = bflo(gw[i][z]), hi = bfhi(gw[i][z]);
        sa[2 * z] += ca * lo; sa[2 * z + 1] += ca * hi; sb2[2 * z] += cs * lo; sb2[2 * z + 1] += cs * hi;
      }
    }
    const u32 a = L_G2R + T4 * 512 + ((lane & 7) * 8 + 4 * hh + 2 * g2 + ((lane >> 3) & 1)) * 4;
    ldswf(a, red8(sa, lane)); ldswf(a + 256, red8(sb2, lane));
  } else {
    if (wave == 4) { poll_n(ws, WS_FLAG + (0 * 16 + J) * 512, 64, tag, lane); lds_post(L_SYNC + 28, 1u); } else lds_wait(L_SYNC + 28, 1u);
    WST(2);
    const int t2 = tid - 256;
    v4u pv[16];
    const u32 c16 = t2 < 210 ? t2 * 16 : OOB;
#pragma unroll
    for (int i = 0; i < 16; ++i) pv[i] = ldb128c(rs_ws, c16, WS_KVP + (J * 16 + i) * KVP_STRIDE);
    u32 la[2];
#pragma unroll
    for (int z = 0; z < 2; ++z) { const int c = t2 < 210 ? 2 * t2 + z : 0, hd = c >= 210, rem = c - hd * 210, fq = rem / 21; la[z] = opq(L_KVB + hd * 1680 + (rem - fq * 21) * 80 + fq * 8); }
    SB();
    float sm[8] = {};
#pragma unroll
    for (int i = 0; i < 16; ++i)
#pragma unroll
      for (int e = 0; e < 4; ++e) { sm[2 * e] += bflo(pv[i][e]); sm[2 * e + 1] += bfhi(pv[i][e]); }
    if (t2 < 210) { ldsw64(la[0], v2u{pkbf(sm[0], sm[1]), pkbf(sm[2], sm[3])}); ldsw64(la[1], v2u{pkbf(sm[4], sm[5]), pkbf(sm[6], sm[7])}); }
  }
  STAMP(7); STAMP(8);
  WST(10);
  bar();
  STAMP(9);
  {
    const bool act = (t16 >> 3) == hh;
    const int hf2 = q4 & 1;
    const u32 kb = L_KVB + hh * 1680;
    const u32 a01 = kb + (4 * hh + 8 * hf2 + p4) * 80, a2 = kb + (hf2 ? 20 : (hh ? 0 : 16) + p4) * 80;
    v4f d[3] = {};
    const v4u b0 = {pq[0], pq[1], pq[2], pq[3]}, b1 = {pq[4], pq[5], pq[6], pq[7]}, b2 = {pq[8], pq[9], 0u, 0u};
#pragma unroll
    for (int T = 0; T < 3; ++T) {
      const u32 r = T < 2 ? a01 + T * 320 : a2;
      const v4u k0 = ldsr128(act ? r + g2 * 48 : L_ZERO), k1 = ldsr128(act ? r + 16 + g2 * 48 : L_ZERO);
      const v2u k2 = ldsr64(act ? r + 32 + g2 * 8 : L_ZERO);
      d[T] = mfma16(asbf(k0), asbf(b0), d[T]); d[T] = mfma16(asbf(k1), asbf(b1), d[T]); d[T] = mfma16(asbf(v4u{k2[0], k2[1], 0u, 0u}), asbf(b2), d[T]);
    }
    const auto dn = __builtin_amdgcn_permlane16_swap(__float_as_uint(d[2][0]), __float_as_uint(d[2][0]), false, false);
    const float rd = 1.0f / (__uint_as_float(dn[1]) + 1e-6f);
    const u32 ob = WS_O + ((8 * P + wave) * 80 + 5 * J) * 256 + t16 * 16;
    stb128c(rs_ws, ob + (3 * hh + g2) * 256, v4u{pkbf(d[0][0] * rd, d[0][1] * rd), pkbf(d[0][2] * rd, d[0][3] * rd), pkbf(d[1][0] * rd, d[1][1] * rd), pkbf(d[1][2] * rd, d[1][3] * rd)});
    stb64c(rs_ws, g2 ? OOB : ob + 512 + hh * 8, v2u{pkbf(d[2][0] * rd, d[2][1] * rd), pkbf(d[2][2] * rd, d[2][3] * rd)});
  }
  publish(ws, WS_FLAG + (1 * 16 + P) * 512 + J * 4, tag);
  STAMP(10);
  R20 = sopq(R20);
  const u32 gus = WS_GU + J * 286720;
  const int tokE = 128 * P + 64 * TH + 16 * T4 + t16;
  v4u xr[3], gq[4];
  const int ge = lane < 40 ? lane : 39;
  const u32 gof = WS_MODG + ((wave == 1 ? 1280 : wave == 2 ? 3200 : 2560) + CT * 80 + 2 * ge) * 8;
  if (dog2 && wave == 7) {
    const u32 a = L_G2R + lane * 4;
    const float av = (ldsrf(a) + ldsrf(a + 512)) + (ldsrf(a + 1024) + ldsrf(a + 1536));
    const float bv = (ldsrf(a + 256) + ldsrf(a + 768)) + (ldsrf(a + 1280) + ldsrf(a + 1792));
    stb64c(rs_ws, WS_AB + (g2k * 3456 + g2c * 64 + lane) * 16, v2u{__float_as_uint(av), tag});
    stb64c(rs_ws, WS_AB + (g2k * 3456 + g2c * 64 + lane) * 16 + 8, v2u{__float_as_uint(bv), tag});
  }
  v2u xq[5];
  if (wave >= 1) {
    if (wave < 4) {
#pragma unroll
      for (int i = 0; i < 4; ++i) gq[i] = ldb128c(rs_ws, gof, i * 30720);
    } else {
#pragma unroll
      for (int i = 0; i < 3; ++i) { const int it = i * 64 + lane < 160 ? i * 64 + lane : 159, tk = it / 10; xr[i] = ldb128(rs_x, (128 * P + 64 * TH + 16 * T4 + tk) * 1280 + (it - tk * 10) * 16, CT * 160); }
    }
#pragma unroll 1
    for (int e = wave - 1; e < 100; e += 7) wo1(e);
    if (wave < 4) {
      if (!tags4(gq[0], gq[1], gq[2], gq[3], tag)) gather4(rs_ws, gof, 30720, tag, gq[0], gq[1], gq[2], gq[3]);
      *(LAS v2f*)(size_t)(L_WPHI + (wave - 1) * 320 + ge * 8) = sum4(gq[0], gq[1], gq[2], gq[3], wave == 3 ? 1.f : 0.f);
    } else {
      const u32 xb = 102400 + T4 * 2816;
#pragma unroll
      for (int i = 0; i < 3; ++i) { const int it = i * 64 + lane < 160 ? i * 64 + lane : 159, tk = it / 10; ldsw128(xb + tk * 176 + (it - tk * 10) * 16, xr[i]); }
#pragma unroll
      for (int t = 0; t < 5; ++t) { xq[t] = ldsr64(xb + t16 * 176 + t * 32 + g4 * 8); asm volatile("" : "+v"(xq[t])); }
    }
    drain();
  }
  SB();
  WST(11);
  if (wave == 0) poll_n(ws, WS_FLAG + (1 * 16 + P) * 512, 16, tag, lane);
  bar();
  STAMP(11);
  auto gu1 = [&](int e8) {
    const u32 src = e8 < 160 ? wrap(e8 + 8 * R20, 160) + wave : 160 + wrap(e8 - 160 + wave + 6 * R20, 120);
    dma16c(rs_ws, vlin, gus + src * 1024, ((e8 + 104) % 144 + wave) * 1024);
  };
  u32 kb2[3];
  kb2[0] = opq(vlin + K2 * 5120); kb2[1] = opq(vlin + K2 * 5120 + 51200); kb2[2] = opq(vlin + K2 * 5120 + 102400);
  auto frag2 = [&](u32 off) { return asbf(ldsr128(kb2[off / 51200] + off % 51200)); };
  float x1[20];
  {
    v4u of[10];
    const u32 ab = WS_O + ((8 * P + 4 * TH + T4) * 80 + 4 * K2) * 256 + vlin;
    sfor<0, 10>([&](auto S) { aloadc<(S.v & 1) * 2048>(of[S.v], rs_ws, ab, (S.v >> 1) * 4096); });
    sfor<0, 5>([&](auto I) { gu1(8 * I.v); });
    v8bf wf[10];
    sfor<0, 10>([&](auto F) { wf[F.v] = frag2((F.v / 5) * 10240 + (F.v % 5) * 1024); });
    v4f acc[5] = {};
    SB();
    sfor<0, 10>([&](auto S) {
      constexpr int s = S.v;
      await1<14 - s>(of[s]);
      sfor<0, 5>([&](auto T) {
        constexpr int t = T.v, f = (5 * s + t) % 10;
        acc[t] = mfma16(wf[f], asbf(of[s]), acc[t]);
        if constexpr (s < 8) wf[f] = frag2((s + 2) * 10240 + t * 1024);
        SB();
      });
    });
    asm volatile("" : "+v"(acc[0]), "+v"(acc[1]), "+v"(acc[2]), "+v"(acc[3]), "+v"(acc[4]));
    WST(12);
    bar();
    const u32 xb = 81920 + T4 * 5120 + vlin;
    if (K2 == 0) {
#pragma unroll
      for (int t = 0; t < 5; ++t) ldsw128f(xb + t * 1024, acc[t]);
    }
    bar();
    if (K2 == 1) {
      float sum = 0.f;
#pragma unroll
      for (int t = 0; t < 5; ++t) {
        const v4f a = acc[t] + ldsr128f(xb + t * 1024);
        const v4f ga = ldsr128f(L_WPHI + t * 64 + g4 * 16), A2 = ldsr128f(L_WPHI + 640 + t * 64 + g4 * 16);
        const u32 p0 = pkbf(bflo(xq[t][0]) + ga[0] * a[0], bfhi(xq[t][0]) + ga[1] * a[1]);
        const u32 p1 = pkbf(bflo(xq[t][1]) + ga[2] * a[2], bfhi(xq[t][1]) + ga[3] * a[3]);
        x1[4 * t] = bflo(p0); x1[4 * t + 1] = bfhi(p0); x1[4 * t + 2] = bflo(p1); x1[4 * t + 3] = bfhi(p1);
        sum += (x1[4 * t] + x1[4 * t + 1]) + (x1[4 * t + 2] + x1[4 * t + 3]);
        const v2u uu = {pkbf(x1[4 * t] * A2[0], x1[4 * t + 1] * A2[1]), pkbf(x1[4 * t + 2] * A2[2], x1[4 * t + 3] * A2[3])};
        stb64c(rs_ws, WS_U + ((tokE >> 4) * 80 + CT * 10 + 2 * t + hh) * 256 + t16 * 16 + g2 * 8, uu);
      }
      const float mt = xsum32(xsum16(sum)) * (1.f / 80.f);
      float m2 = 0.f;
#pragma unroll
      for (int i = 0; i < 20; ++i) { const float dd = x1[i] - mt; m2 += dd * dd; }
      m2 = xsum32(xsum16(m2));
      stb64c(rs_ws, g4 == 0 ? WS_STAT + ((P * 8 + CT) * 128 + (tokE & 127)) * 8 : OOB, v2u{__float_as_uint(mt), __float_as_uint(m2)});
      drain();
      if (lane == 0) setflag(ws, WS_FLAG + (2 * 16 + P) * 512 + (J * 4 + T4) * 4, tag);
#pragma unroll 1
      for (int z = 0; z < 6; ++z) { const int e = z < 5 ? 120 + 5 * T4 + z : 140 + T4; dma16c(rs_ws, vlin, gus + wrap(e + 8 * R20, 160) * 1024, (e - 40) * 1024); }
    } else {
#pragma unroll
      for (int i = 0; i < 20; ++i) x1[i] = 0.f;
      {
        const int abe = T4 * 56 + (lane < 56 ? lane : 55), mat = abe >= 112, c = abe - 112 * mat;
        v4u g0, g1, g2v, g3;
        gather4(rs_ws, WS_AB + (mat * 1728 + (t0 * 16 + c < 1728 ? t0 * 16 + c : 1727)) * 16, 3456 * 16, tag, g0, g1, g2v, g3);
        ldswf(L_AB + abe * 4, (__uint_as_float(g0[0]) + __uint_as_float(g1[0])) + (__uint_as_float(g2v[0]) + __uint_as_float(g3[0])));
        ldswf(L_AB + 896 + abe * 4, (__uint_as_float(g0[2]) + __uint_as_float(g1[2])) + (__uint_as_float(g2v[2]) + __uint_as_float(g3[2])));
      }
      if (wave > 0) {
#pragma unroll 1
        for (int e = 39 + wave; e < 120; e += 3) { dma16c(rs_ws, vlin, gus + wrap(e + 8 * R20, 160) * 1024, (e - 40) * 1024); waitv<GU_THR>(); }
      }
    }
  }
  STAMP(12);
  WST(13);
  const u32 wds = WS_WD + CT * 276480;
  v4u af[27];
  const u32 ab4 = WS_ACT + ((8 * P + 4 * TH + T4) * 216 + 4 * K2) * 256 + vlin;
  auto lda = [&](auto N) { constexpr int n = N.v; aloadc<0>(af[n], rs_ws, ab4, (n < 16 ? wrap(n + R4A, 16) : 16 + wrap(n - 16 + R4B, 11)) * 2048); };
  auto wflag = [&](int hop) { bflag(rs_ws, tag, WS_FLAG + (hop * 16 + P) * 512 + (wave * 16 + J) * 4); };
  auto wd1 = [&](int e) { dma16c(rs_ws, vlin, wds + (e < 160 ? wrap(e + 10 * R4A, 160) : 160 + wrap(e - 160 + 10 * R4B, 110)) * 1024, wrap(wrap(e + 100, 150), 150) * 1024); };
  {
    if (wave == 0) poll_n(ws, WS_FLAG + (2 * 16 + P) * 512, 64, tag, lane);
    bar();
    STAMP(13);
    R20 = sopq(R20);
    v4u uf[20], st;
    const u32 uvo = WS_U + (8 * P + wave) * 20480 + vlin;
    constexpr int U0 = 8;
    auto ldu = [&](auto N) { aloadc<0>(uf[N.v], rs_ws, uvo, wrap(N.v + R20, 20) * 1024); };
    sfor<0, U0>(ldu);
    const u32 stoff = WS_STAT + ((P * 8 + 2 * g4 + (lane & 1)) * 128 + 16 * wave + (t16 & ~1)) * 8;
    constexpr auto sl = [](int e) { return (u32)(((e + 104) % 144) * 1024); };
    v8bf wf[8];
    sfor<0, 8>([&](auto F) { wf[F.v] = frag(sl(F.v)); });
    v4f aA[8] = {}, aB[6] = {};
    constexpr auto nops = [](int m) { return (m < 13 ? 1 : 0) + (m >= 8 ? 1 : 0); };
    constexpr auto sum = [=](int a, int b2) { int c = 0; for (int m = a; m < b2; ++m) c += nops(m); return c; };
    SB();
    sfor<0, 20>([&](auto N) {
      constexpr int n = N.v;
      if constexpr (n == 2 || n == 8 || n == 13) { rawbar(); SB(); }
      if constexpr (n == 17) { await1<3>(st); rawbar(); SB(); }
      await1<(n < U0 ? U0 - 1 - n + sum(0, n) : nops(n - U0) - 1 + sum(n - U0 + 1, n))>(uf[n]);
      sfor<0, 8>([&](auto T) {
        constexpr int t = T.v;
        aA[t] = mfma16(wf[t], asbf(uf[n]), aA[t]);
        wf[t] = frag(sl(8 * n + 8 + t));
        if constexpr (t == 0) {
          if constexpr (n + U0 < 20) ldu(IC<(n + U0 < 20 ? n + U0 : 0)>{});
          else if constexpr (n == 12) aloadc<0>(st, rs_ws, stoff, 0);
        }
        if constexpr (t == 4 && n >= 8) gu1(144 + 8 * (n - 8));
        SB();
      });
    });
    asm volatile("" : "+v"(aA[0]), "+v"(aA[1]), "+v"(aA[2]), "+v"(aA[3]), "+v"(aA[4]), "+v"(aA[5]), "+v"(aA[6]), "+v"(aA[7]));
    STAMP(14);
    float rs2, mr2;
    {
      const bool od = lane & 1;
      const float ma = __uint_as_float(od ? st[2] : st[0]), qa = __uint_as_float(od ? st[3] : st[1]);
      const float mb = __uint_as_float(__builtin_amdgcn_update_dpp(0u, od ? st[0] : st[2], 0xb1, 0xf, 0xf, true));
      const float qb = __uint_as_float(__builtin_amdgcn_update_dpp(0u, od ? st[1] : st[3], 0xb1, 0xf, 0xf, true));
      const float m = xsum32(xsum16(ma + mb)) * 0.125f;
      const float q = qa + qb + 80.f * ((ma - m) * (ma - m) + (mb - m) * (mb - m));
      rs2 = __builtin_amdgcn_rsqf(xsum32(xsum16(q)) * (1.f / DM) + 1e-5f); mr2 = -m * rs2;
    }
    auto geglu4 = [&](const v4f& g, const v4f& u, u32 cb) {
      const v4f ag = ldsr128f(cb), au = ldsr128f(cb + 448), bg = ldsr128f(cb + 896), bu = ldsr128f(cb + 1344);
      const v4f gv = g * rs2 + (ag * mr2 + bg), uv = u * rs2 + (au * mr2 + bu);
      const v4f z = gv * (gv * gv * (-0.044715f * 2.302208198f) + (-2.302208198f));
      v4f d = {__builtin_amdgcn_exp2f(z[0]), __builtin_amdgcn_exp2f(z[1]), __builtin_amdgcn_exp2f(z[2]), __builtin_amdgcn_exp2f(z[3])};
      d += 1.f;
      const v4f r = {__builtin_amdgcn_rcpf(d[0]), __builtin_amdgcn_rcpf(d[1]), __builtin_amdgcn_rcpf(d[2]), __builtin_amdgcn_rcpf(d[3])};
      const v4f o = gv * uv * r;
      return v2u{pkbf(o[0], o[1]), pkbf(o[2], o[3])};
    };
    const u32 abm = opq(L_AB + g4 * 32);
    const u32 ob = WS_ACT + (8 * P + wave) * 55296;
    auto epiA = [&]() {
#pragma unroll
      for (int bk = 0; bk < 2; ++bk) {
        const v2u lo = geglu4(aA[4 * bk], aA[4 * bk + 2], abm + bk * 128), hi = geglu4(aA[4 * bk + 1], aA[4 * bk + 3], abm + bk * 128 + 16);
        astorec(v4u{lo[0], lo[1], hi[0], hi[1]}, rs_ws, vlin, ob + (2 * J + bk) * 1024);
      }
    };
    auto loopB = [&](auto N0, auto N1) {
      sfor<6 * N0.v, 6 * N1.v>([&](auto I) {
        constexpr int i = I.v, sb = i / 6;
        aB[i % 6] = mfma16(wf[i % 8], asbf(uf[sb]), aB[i % 6]);
        if constexpr (i + 8 < 120) wf[i % 8] = frag(sl(160 + i + 8));
        if constexpr (sb < 5 && i % 6 == 3) gu1(240 + 8 * sb);
        if constexpr (sb >= 5 && sb < 8 && i % 6 == 3) { if constexpr (sb < 7) wd1(8 * (sb - 5) + wave); else wd1(16 + (wave & 3)); }
        if constexpr (sb >= 10 && sb < 14 && i % 3 == 1) wd1(20 + 8 * ((i - 60) / 3) + wave);
        if constexpr (sb >= 15 && sb < 19 && i % 6 == 1) wd1(84 + 8 * (sb - 15) + wave);
        if constexpr (sb >= 15 && i % 6 == 4) lda(IC<(sb >= 15 ? sb - 15 : 0)>{});
        SB();
      });
    };
    auto pinB = [&]() { asm volatile("" : "+v"(aB[0]), "+v"(aB[1]), "+v"(aB[2]), "+v"(aB[3]), "+v"(aB[4]), "+v"(aB[5])); };
    epiA();
    SB();
    loopB(IC<0>{}, IC<4>{});
    waitv<6>();
    rawbar();
    loopB(IC<4>{}, IC<7>{});
    waitv<7>();
    wflag(3);
    loopB(IC<7>{}, IC<10>{});
    waitv<4>();
    rawbar();
    STAMP(15);
    loopB(IC<10>{}, IC<15>{});
    pinB();
    if (wave == 0) poll_n(ws, WS_FLAG + (3 * 16 + P) * 512 + TH * 256, 64, tag, lane);
    rawbar();
    loopB(IC<15>{}, IC<20>{});
    pinB();
    STAMP(16);
    {
      const v2u lo = geglu4(aB[0], aB[2], abm + 256), hi = geglu4(aB[1], aB[3], abm + 272);
      astorec(v4u{lo[0], lo[1], hi[0], hi[1]}, rs_ws, vlin, ob + (32 + J) * 1024);
      const v2u hf4 = geglu4(aB[4], aB[5], L_AB + 384 + g4 * 16);
      __builtin_amdgcn_raw_buffer_store_b64(hf4, rs_ws, nt == 7 ? hh * 256 + t16 * 16 + g2 * 8 : OOB, ob + 48 * 1024 + (J - (J >> 2)) * 512, AUX_SC1);
    }
  }
  WST(14);
  sfor<5, 10>(lda);
  await4<7>(af[0], af[1], af[2], af[3]);
  asm volatile("" : "+v"(af[4]));
  rawbar();
  STAMP(17);
  {
    constexpr auto sl = [](int n, int t) { return (u32)(((n + 10) % 15) * 10240 + t * 1024); };
    v8bf wf[10];
    sfor<0, 10>([&](auto F) { wf[F.v] = frag2(sl(F.v / 5, F.v % 5)); });
    v4f acc[5] = {};
    constexpr auto nops = [](int m) { return ((m < 6 || (m >= 8 && m <= 18)) ? 1 : 0) + (m < 20 ? 1 : 0) + (m == 2 ? 1 : 0); };
    constexpr auto sum = [=](int a, int b2) { int c = 0; for (int m = a; m < b2; ++m) c += nops(m); return c; };
    SB();
    sfor<0, 27>([&](auto N) {
      constexpr int n = N.v;
      if constexpr (n == 2) { waitv<5 + sum(0, 2)>(); wflag(4); }
      if constexpr (n == 8) { if (wave == 0) poll_n(ws, WS_FLAG + (4 * 16 + P) * 512 + TH * 256, 64, tag, lane); }
      if constexpr (n >= 4 && n <= 24 && (n & 1) == 0) { if constexpr (n >= 8) waitv<sum(n - 4, n)>(); rawbar(); SB(); }
      if constexpr (n >= 5 && n < 10) await1<9 - n + sum(0, n)>(af[n]);
      if constexpr (n >= 10 && n < 16) await1<1 + sum(n - 9, n)>(af[n]);
      if constexpr (n >= 16) await1<1 + sum(n - 7, n)>(af[n]);
      sfor<0, 5>([&](auto T) {
        constexpr int t = T.v, f = (5 * n + t) % 10;
        acc[t] = mfma16(wf[f], asbf(af[n]), acc[t]);
        if constexpr (n < 25) wf[f] = frag2(sl(n + 2, t));
        if constexpr (n < 6 && t == 0) lda(IC<(n < 6 ? n + 10 : 0)>{});
        if constexpr (n >= 8 && n <= 18 && t == 0) lda(IC<(n <= 18 ? n + 8 : 0)>{});
        if constexpr (n < 20 && t == 2) { const int e = 116 + 8 * n + wave; wd1(n == 19 ? (e < 270 ? e : 269) : e); }
        SB();
      });
      if constexpr (n == 7) asm volatile("" : "+v"(acc[0]), "+v"(acc[1]), "+v"(acc[2]), "+v"(acc[3]), "+v"(acc[4]));
    });
    asm volatile("" : "+v"(acc[0]), "+v"(acc[1]), "+v"(acc[2]), "+v"(acc[3]), "+v"(acc[4]));
    STAMP(18);
    const u32 xb = 81920 + T4 * 5120 + vlin;
    if (K2 == 0) {
#pragma unroll
      for (int t = 0; t < 5; ++t) ldsw128f(xb + t * 1024, acc[t]);
    }
    bar();
    if (K2 == 1) {
      const u32 ob = 102400 + T4 * 2816;
#pragma unroll
      for (int t = 0; t < 5; ++t) {
        const v4f a = acc[t] + ldsr128f(xb + t * 1024);
        const v4f gb = ldsr128f(L_WPHI + 320 + t * 64 + g4 * 16);
        ldsw64(ob + t16 * 176 + t * 32 + g4 * 8, v2u{pkbf(x1[4 * t] + gb[0] * a[0], x1[4 * t + 1] + gb[1] * a[1]), pkbf(x1[4 * t + 2] + gb[2] * a[2], x1[4 * t + 3] + gb[3] * a[3])});
      }
#pragma unroll
      for (int i = 0; i < 3; ++i) {
        const int it = i * 64 + lane, tk = it / 10, ch = it - tk * 10;
        if (it < 160) *(GAS v4u*)((GAS char*)out + (128 * P + 64 * TH + 16 * T4 + tk) * 1280 + CT * 160 + ch * 16) = ldsr128(ob + tk * 176 + ch * 16);
      }
    }
  }
  if (b == 0 && tid == 0) __hip_atomic_store((GAS u32*)(ws + WS_EPOCH), tag, RLX_AGENT);
  WST(15);
  STAMP(19);
}
}

extern "C" void kernel_launch(void* const* d_in, const int* in_sizes, int n_in, void* d_out, int out_size, void* d_ws, size_t ws_size, hipStream_t stream) {
  (void)in_sizes; (void)n_in; (void)out_size; (void)ws_size;
  (void)hipFuncSetAttribute((const void*)fused_kernel, hipFuncAttributeMaxDynamicSharedMemorySize, L_TOTAL);
  fused_kernel<<<256, 512, L_TOTAL, stream>>>((const u16*)d_in[0], (const u16*)d_in[1], (const u16*)d_in[2], (const u16*)d_in[3], (const u16*)d_in[5],
                                              (const u16*)d_in[6], (const u16*)d_in[7], (const u16*)d_in[8], (const u16*)d_in[9], (u16*)d_out, (char*)d_ws);
}
```

```cpp
#include <hip/hip_runtime.h>
#include <stdint.h>

typedef unsigned short u16;
typedef unsigned u32;
typedef unsigned long long u64;
typedef u32 v4u __attribute__((ext_vector_type(4)));
typedef u32 v2u __attribute__((ext_vector_type(2)));
typedef float v4f __attribute__((ext_vector_type(4)));
typedef float v2f __attribute__((ext_vector_type(2)));
typedef float v16f __attribute__((ext_vector_type(16)));
typedef short v4s __attribute__((ext_vector_type(4)));
typedef __bf16 v8bf __attribute__((ext_vector_type(8)));
typedef __bf16 v2bf __attribute__((ext_vector_type(2)));
typedef _Float16 v2h __attribute__((ext_vector_type(2)));
#define LAS __attribute__((address_space(3)))
#define GAS __attribute__((address_space(1)))
#define DI __device__ __forceinline__
#define RLX_AGENT __ATOMIC_RELAXED, __HIP_MEMORY_SCOPE_AGENT
#define SB() __builtin_amdgcn_sched_barrier(0)
typedef __amdgpu_buffer_rsrc_t rsrc_t;

#define ROT_ON 1
#define NTS " nt"
namespace {
constexpr int SEQ = 2048, DM = 640, FF = 1728;
constexpr u32 WS_EPOCH = 0;
constexpr u32 WS_FLAG = 1024;
constexpr u32 WS_ST1 = 61440;
constexpr u32 WS_MODG = WS_ST1 + 32768;
constexpr u32 WS_AB = WS_MODG + 4 * 30720;
constexpr u32 WS_KVP = WS_AB + 4 * 3456 * 16;
constexpr u32 KVP_STRIDE = 3456;
constexpr u32 WS_STAT = WS_KVP + 256 * KVP_STRIDE;
constexpr u32 WS_O = WS_STAT + SEQ * 64;
constexpr u32 WS_U = WS_O + SEQ * DM * 2;
constexpr u32 WS_ACT = WS_U + SEQ * DM * 2;
constexpr u32 WS_GU = WS_ACT + SEQ * FF * 2;
constexpr u32 WS_WD = WS_GU + 16 * 286720;
constexpr u32 WS_WQ = WS_WD + 8 * 276480;
constexpr u32 WS_WO = WS_WQ + 16 * 153600;
constexpr u32 WS_T = WS_WO + 16 * 102400;
constexpr u32 WS_END = WS_T + SEQ * DM * 2;
constexpr u32 WS_DBG = (WS_END + 4095) & ~4095u;
static_assert(WS_KVP % 128 == 0 && WS_STAT % 128 == 0 && WS_O % 256 == 0, "align");
constexpr u32 L_RQ = 102400;
constexpr u32 L_RAW = 102400;
constexpr u32 L_MISC = 153600;
constexpr u32 L_COA = L_MISC, L_COS = L_MISC + 2560;
constexpr u32 L_KVB = L_MISC + 5120;
constexpr u32 L_ZERO = L_KVB + 3360;
constexpr u32 L_WPHI = L_ZERO + 16;
constexpr u32 L_SYNC = L_WPHI + 1600;
constexpr u32 L_TOTAL = 163840;
constexpr u32 L_G2A = L_COA, L_G2S = L_COA + 640;
constexpr u32 L_G2R = L_COA + 1280;
constexpr u32 L_AB = L_COA + 3328;
static_assert(L_SYNC + 64 <= L_TOTAL && L_AB + 1792 <= L_KVB, "lds");
constexpr u32 L_WO = 32768;
constexpr int WO_EARLY = 48, KV_THR = 8, GU_THR = 6;
#define CODE_PF 53248

#define CODE_BACK 0
constexpr u32 SPIN_CAP = 1u << 15;
constexpr int AUX_SC1 = 16;
constexpr u32 OOB = 0x7ffffff0u;

DI float bflo(u32 v) { return __uint_as_float(v << 16); }
DI float bfhi(u32 v) { return __uint_as_float(v & 0xffff0000u); }
DI u32 pkbf(float a, float b) { v2f t = {a, b}; return __builtin_bit_cast(u32, __builtin_convertvector(t, v2bf)); }
DI float rbf(float a) { return bflo(pkbf(a, a)); }
DI void bar() { asm volatile("s_waitcnt lgkmcnt(0)\n\ts_barrier" ::: "memory"); }
DI void rawbar() { asm volatile("s_barrier" ::: "memory"); }
DI void drain() { asm volatile("s_waitcnt vmcnt(0)" ::: "memory"); }
template <int N> DI void waitv() { asm volatile("s_waitcnt vmcnt(%0)" ::"i"(N) : "memory"); }
template <int I> struct IC { static constexpr int v = I; };
template <int B, int E, class F> DI void sfor(F&& f) { if constexpr (B < E) { f(IC<B>{}); sfor<B + 1, E>(f); } }
DI rsrc_t mkrs(const void* p, u32 bytes) { return __builtin_amdgcn_make_buffer_rsrc((void*)p, 0, bytes, 0x00020000); }
DI void dma16(rsrc_t rs, u32 voff, u32 soff, u32 lds) {
  asm volatile("s_mov_b32 m0, %0\n\ts_nop 0\n\tbuffer_load_dwordx4 %1, %2, %3 offen lds" ::"s"(lds), "v"(voff), "s"(rs), "s"(soff) : "memory");
}
template <int NL> DI void dmam(rsrc_t rs, u32 voff, u32 soff, u32 lds) {
  asm volatile("s_mov_b32 m0, %0\n\ts_mov_b32 exec_hi, %4\n\tbuffer_load_dwordx4 %1, %2, %3 offen" NTS " lds\n\ts_mov_b32 exec_hi, -1"
               ::"s"(lds), "v"(voff), "s"(rs), "s"(soff), "i"((1u << (NL - 32)) - 1u) : "memory");
}
DI void dma16c(rsrc_t rs, u32 voff, u32 soff, u32 lds) {
  asm volatile("s_mov_b32 m0, %0\n\ts_nop 0\n\tbuffer_load_dwordx4 %1, %2, %3 offen sc1 lds" ::"s"(lds), "v"(voff), "s"(rs), "s"(soff) : "memory");
}
template <int NL> DI void dmamc(rsrc_t rs, u32 voff, u32 soff, u32 lds) {
  asm volatile("s_mov_b32 m0, %0\n\ts_mov_b32 exec_hi, %4\n\tbuffer_load_dwordx4 %1, %2, %3 offen sc1 lds\n\ts_mov_b32 exec_hi, -1"
               ::"s"(lds), "v"(voff), "s"(rs), "s"(soff), "i"((1u << (NL - 32)) - 1u) : "memory");
}
DI u32 opq(u32 v) { asm volatile("" : "+v"(v)); return v; }
DI v4s trd(u32 addr) { return __builtin_amdgcn_ds_read_tr16_b64_v4i16((LAS v4s*)(size_t)addr); }
DI v8bf trfrag(u32 a0, u32 a1) {
  v4s a = trd(a0), b = trd(a1);
  return __builtin_bit_cast(v8bf, __builtin_shufflevector(a, b, 0, 1, 2, 3, 4, 5, 6, 7));
}
DI v16f mfma32(v8bf a, v8bf b, v16f c) { return __builtin_amdgcn_mfma_f32_32x32x16_bf16(a, b, c, 0, 0, 0); }
DI v4f mfma16(v8bf a, v8bf b, v4f c) { return __builtin_amdgcn_mfma_f32_16x16x32_bf16(a, b, c, 0, 0, 0); }
DI v8bf asbf(v4u v) { return __builtin_bit_cast(v8bf, v); }
DI v4u ldsr128(u32 a) { return *(LAS v4u*)(size_t)a; }
DI v4f ldsr128f(u32 a) { return *(LAS v4f*)(size_t)a; }
DI v2u ldsr64(u32 a) { return *(LAS v2u*)(size_t)a; }
DI void ldsw128(u32 a, v4u v) { *(LAS v4u*)(size_t)a = v; }
DI void ldsw128f(u32 a, v4f v) { *(LAS v4f*)(size_t)a = v; }
DI void ldsw64(u32 a, v2u v) { *(LAS v2u*)(size_t)a = v; }
DI float ldsrf(u32 a) { return *(LAS float*)(size_t)a; }
DI void ldswf(u32 a, float v) { *(LAS float*)(size_t)a = v; }
DI float mixlo(u32 t, float a, float s) { float r; asm("v_fma_mix_f32 %0, %1, %2, %3 op_sel_hi:[1,0,0]" : "=v"(r) : "v"(t), "v"(a), "v"(s)); return r; }
DI float mixhi(u32 t, float a, float s) { float r; asm("v_fma_mix_f32 %0, %1, %2, %3 op_sel:[1,0,0] op_sel_hi:[1,0,0]" : "=v"(r) : "v"(t), "v"(a), "v"(s)); return r; }
DI float xsum16(float v) {
  auto r = __builtin_amdgcn_permlane16_swap(__float_as_uint(v), __float_as_uint(v), false, false);
  return __uint_as_float(r[0]) + __uint_as_float(r[1]);
}
DI float xsum32(float v) {
  auto r = __builtin_amdgcn_permlane32_swap(__float_as_uint(v), __float_as_uint(v), false, false);
  return __uint_as_float(r[0]) + __uint_as_float(r[1]);
}
template <int CTRL> DI float dppadd(float v) {
  return v + __uint_as_float(__builtin_amdgcn_update_dpp(0u, __float_as_uint(v), CTRL, 0xf, 0xf, true));
}
constexpr int DPP_ROR4 = 0x124, DPP_ROR8 = 0x128;
DI float red8(const float (&a)[8], int lane) {
  float b4[4], c[2];
#pragma unroll
  for (int e = 0; e < 4; ++e) { auto r = __builtin_amdgcn_permlane32_swap(__float_as_uint(a[e]), __float_as_uint(a[4 + e]), false, false); b4[e] = __uint_as_float(r[0]) + __uint_as_float(r[1]); }
#pragma unroll
  for (int e = 0; e < 2; ++e) { auto r = __builtin_amdgcn_permlane16_swap(__float_as_uint(b4[e]), __float_as_uint(b4[2 + e]), false, false); c[e] = __uint_as_float(r[0]) + __uint_as_float(r[1]); }
  const float x = dppadd<DPP_ROR8>(c[0]), y = dppadd<DPP_ROR8>(c[1]);
  return (lane & 8) ? y : x;
}
DI v4u ldb128(rsrc_t rs, u32 vo, u32 so) { return __builtin_amdgcn_raw_buffer_load_b128(rs, vo, so, 2); }
DI v4u ldb128c(rsrc_t rs, u32 vo, u32 so) { return __builtin_amdgcn_raw_buffer_load_b128(rs, vo, so, AUX_SC1); }
template <int OFF> DI void aload(v4u& r, rsrc_t rs, u32 vo, u32 so) {
  asm volatile("buffer_load_dwordx4 %0, %1, %2, %3 offen offset:%4" NTS : "=v"(r) : "v"(vo), "s"(rs), "s"(so), "i"(OFF) : "memory");
}
template <int OFF> DI void aloadc(v4u& r, rsrc_t rs, u32 vo, u32 so) {
  asm volatile("buffer_load_dwordx4 %0, %1, %2, %3 offen offset:%4 sc1" : "=v"(r) : "v"(vo), "s"(rs), "s"(so), "i"(OFF) : "memory");
}
template <int N> DI void await1(v4u& a) { asm volatile("s_waitcnt vmcnt(%1)" : "+v"(a) : "i"(N) : "memory"); }
template <int N> DI void await2(v4u& a, v4u& b) { asm volatile("s_waitcnt vmcnt(%2)" : "+v"(a), "+v"(b) : "i"(N) : "memory"); }
template <int N> DI void await4(v4u& a, v4u& b, v4u& c, v4u& d) { asm volatile("s_waitcnt vmcnt(%4)" : "+v"(a), "+v"(b), "+v"(c), "+v"(d) : "i"(N) : "memory"); }
DI void astorec(v4u v, rsrc_t rs, u32 vo, u32 so) { u32 o = vo + so; asm volatile("" : "+v"(o)); __builtin_amdgcn_raw_buffer_store_b128(v, rs, o, 0, AUX_SC1); }
DI int sopq(int v) { asm volatile("" : "+s"(v)); return v; }
template <int N> DI void await3(v4u& a, v4u& b, v4u& c) { asm volatile("s_waitcnt vmcnt(%3)" : "+v"(a), "+v"(b), "+v"(c) : "i"(N) : "memory"); }
DI void stb128c(rsrc_t rs, u32 vo, v4u v) { __builtin_amdgcn_raw_buffer_store_b128(v, rs, vo, 0, AUX_SC1); }
DI void stb64c(rsrc_t rs, u32 vo, v2u v) { __builtin_amdgcn_raw_buffer_store_b64(v, rs, vo, 0, AUX_SC1); }

#define WST(i) do {} while (0)
#define STAMP(i) do {} while (0)

DI void setflag(char* ws, u32 off, u32 tag) { (void)__hip_atomic_exchange((GAS u32*)(ws + off), tag, RLX_AGENT); }
DI void bflag(rsrc_t rs, u32 tag, u32 off) { asm volatile("s_mov_b64 exec, 1\n\tbuffer_atomic_swap %0, off, %2, %1\n\ts_mov_b64 exec, -1" ::"v"(tag), "s"(off), "s"(rs) : "memory"); }
DI void publish(char* ws, u32 flagoff, u32 tag) {
  drain();
  bar();
  if (threadIdx.x == 0) setflag(ws, flagoff, tag);
}
DI void poll_n(char* ws, u32 off, int n, u32 tag, int lane) {
  GAS u32* f = (GAS u32*)(ws + off) + lane;
#pragma unroll 1
  for (u32 spin = 0; spin < SPIN_CAP; ++spin) {
    u32 v = tag;
    if (lane < n) { v = 0; asm volatile("global_atomic_or %0, %1, %0, off sc0 sc1\n\ts_waitcnt vmcnt(0)" : "+v"(v) : "v"(f) : "memory"); }
    if (__all(v == tag)) break;
    __builtin_amdgcn_s_sleep(1);
  }
}
DI void lds_post(u32 a, u32 tag) { asm volatile("" ::: "memory"); *(volatile LAS u32*)(size_t)a = tag; }
DI void lds_wait(u32 a, u32 tag) {
#pragma unroll 1
  for (u32 spin = 0; spin < (SPIN_CAP << 4); ++spin) {
    if (__all(*(volatile LAS u32*)(size_t)a >= tag)) break;
    __builtin_amdgcn_s_sleep(2);
  }
  asm volatile("" ::: "memory");
}
DI void gather2(rsrc_t rs, u32 o1, u32 o2, u32 tag, v4u& g1, v4u& g2) {
#pragma unroll 1
  for (u32 spin = 0; spin < SPIN_CAP; ++spin) {
    asm volatile("" ::: "memory");
    g1 = ldb128c(rs, o1, 0);
    g2 = ldb128c(rs, o2, 0);
    if (__all(g1[1] == tag && g1[3] == tag && g2[1] == tag && g2[3] == tag)) break;
    __builtin_amdgcn_s_sleep(1);
  }
}
DI void gather4(rsrc_t rs, u32 o, u32 st, u32 tag, v4u& g0, v4u& g1, v4u& g2, v4u& g3) {
#pragma unroll 1
  for (u32 spin = 0; spin < SPIN_CAP; ++spin) {
    asm volatile("" ::: "memory");
    g0 = ldb128c(rs, o, 0); g1 = ldb128c(rs, o, st); g2 = ldb128c(rs, o, 2 * st); g3 = ldb128c(rs, o, 3 * st);
    if (__all(g0[1] == tag && g0[3] == tag && g1[1] == tag && g1[3] == tag && g2[1] == tag && g2[3] == tag && g3[1] == tag && g3[3] == tag)) break;
    __builtin_amdgcn_s_sleep(1);
  }
}
DI v2f sum4(const v4u& a, const v4u& b, const v4u& c, const v4u& d, float one) {
  const float x = (__uint_as_float(a[0]) + __uint_as_float(b[0])) + (__uint_as_float(c[0]) + __uint_as_float(d[0]));
  const float y = (__uint_as_float(a[2]) + __uint_as_float(b[2])) + (__uint_as_float(c[2]) + __uint_as_float(d[2]));
  return v2f{rbf(one + rbf(x)), rbf(one + rbf(y))};
}
DI bool tags4(const v4u& a, const v4u& b, const v4u& c, const v4u& d, u32 t) {
  return __all(a[1] == t && a[3] == t && b[1] == t && b[3] == t && c[1] == t && c[3] == t && d[1] == t && d[3] == t);
}
DI v4f unp4(v4u g, float one) { return v4f{rbf(one + bflo(g[0])), rbf(one + bfhi(g[0])), rbf(one + bflo(g[2])), rbf(one + bfhi(g[2]))}; }

DI void softmax20(const float (&p)[20], u32 (&o)[10]) {
  float mx = p[0];
#pragma unroll
  for (int i = 1; i < 20; ++i) mx = fmaxf(mx, p[i]);
  const float L2E = 1.4426950408889634f, a = -mx * L2E;
  float e[20], s0 = 0.f, s1 = 0.f;
#pragma unroll
  for (int i = 0; i < 20; ++i) { e[i] = __builtin_amdgcn_exp2f(__builtin_fmaf(p[i], L2E, a)); if (i & 1) s1 += e[i]; else s0 += e[i]; }
  const float r = __builtin_amdgcn_rcpf(s0 + s1);
#pragma unroll
  for (int i = 0; i < 10; ++i) o[i] = pkbf(e[2 * i] * r, e[2 * i + 1] * r);
}
__global__ __launch_bounds__(512, 2) void fused_kernel(const u16* __restrict__ x, const u16* __restrict__ wqkv, const u16* __restrict__ wphi,
                                                       const u16* __restrict__ wo, const u16* __restrict__ wg, const u16* __restrict__ wu,
                                                       const u16* __restrict__ wd, const u16* __restrict__ cvec, const u16* __restrict__ wmod,
                                                       u16* __restrict__ out, char* ws) {
  const int tid = threadIdx.x, lane = tid & 63;
  const int wave = __builtin_amdgcn_readfirstlane(tid >> 6);
  const int b = blockIdx.x, xcd = b & 7, idx = b >> 3;
  __builtin_assume(wave >= 0 && wave < 8);
  __builtin_assume(b >= 0 && b < 256);
  {
    const rsrc_t rs_cd = mkrs((const void*)(__builtin_amdgcn_s_getpc() & ~(u64)127), CODE_PF);
    asm volatile("s_mov_b32 m0, 0x27000\n\ts_mov_b64 exec, 3\n\tbuffer_load_dword %0, %1, %2 offen lds\n\ts_mov_b64 exec, -1" ::"v"(lane * 32768), "s"(rs_cd), "s"((wave * 32 + idx) * 128) : "memory");
  }
  SB();
  STAMP(0);
  const int T4 = wave & 3, K2 = wave >> 2;
  const int mc = b >> 2, mk = b & 3;
  const u32 coff2 = (160 * mk + 40 * T4 + 5 * (lane >> 3)) * 2;
  const int P = 4 * (xcd >> 1) + (idx >> 3);
  const int J = 8 * (xcd & 1) + (idx & 7);
  const int TH = J >> 3, CT = J & 7;
  const rsrc_t rs_ws = mkrs(ws, WS_END);
  const rsrc_t rs_x = mkrs(x, SEQ * DM * 2);
  const u32 vlin = lane * 16;
  const int hh = lane >> 5, i32 = lane & 31, g4 = lane >> 4, t16 = lane & 15;
  const int g2 = g4 & 1, q4 = t16 >> 2, p4 = lane & 3;
  v4u wm[5], cq, tagq;
  {
    const rsrc_t rs_a = mkrs(wmod, K2 ? 0 : DM * 6 * DM * 2), rs_c = mkrs(cvec, DM * 2);
    const u32 vo = coff2 * 3840 + mc * 128 + (lane & 7) * 16;
    aload<0>(cq, rs_c, coff2 & ~3u, 0);
    sfor<0, 5>([&](auto I) { aload<0>(wm[I.v], rs_a, vo, I.v * 7680); });
    aloadc<0>(tagq, rs_ws, WS_EPOCH, 0);
  }
  SB();
  ldsw128(L_SYNC + (lane & 3) * 16, v4u{0u, 0u, 0u, 0u});
  ldsw128(L_ZERO, v4u{0u, 0u, 0u, 0u});
  const int rot32 = (idx & 7) * 4 + (idx >> 3);
  const int R10 = (rot32 * 10) >> 5;
  int R20 = (rot32 * 20) >> 5;
  const int R4A = rot32 >> 1, R4B = (rot32 * 11) >> 5;
  auto wrap = [](int v, int m) { return v >= m ? v - m : v; };
  const int t0 = 7 * J - (J >> 2), nt = (J & 3) == 3 ? 6 : 7;
  const int rq27 = 2 * P + TH;
  const u32 wqs = WS_WQ + J * 153600;
  auto tile = [&](u32 src, u32 rstride, u32 dst, u32 so) {
    astorec(__builtin_bit_cast(v4u, trfrag(src + q4 * rstride, src + (4 + q4) * rstride)), rs_ws, dst, so);
  };
  const int l10 = lane / 10, c10 = lane - l10 * 10;
  const u32 v10 = l10 * 1280 + c10 * 16;
  constexpr u32 L_R2 = L_RQ + 16384;
  constexpr u32 L_RD = L_R2 + 17920;
  constexpr u32 L_OR = L_RD + 10240;
  static_assert(L_OR + 6400 <= L_MISC, "lds");
  constexpr u32 L_WR = 98304;
  u32 ta[2] = {}, td[2][2] = {};
  if (K2) {
    {
      const rsrc_t rs_w = mkrs(wqkv, DM * 3 * DM * 2), rs_2 = mkrs(T4 == 1 ? wphi : wqkv, T4 == 1 ? 32 * 400 * 2 : DM * 3 * DM * 2);
      const int r = lane / 15, c = lane - r * 15, pt = c / 5;
      const u32 vo = r * 3840 + pt * 1280 + (c - pt * 5) * 16, so = (40 * P + 8 * T4) * 3840 + J * 80;
      dmam<60>(rs_w, vo, so, L_WR + T4 * 1920); dmam<60>(rs_w, vo, so + 4 * 3840, L_WR + T4 * 1920 + 960);
      const u32 v2 = T4 == 1 ? vlin : vo, s2 = T4 == 1 ? J * 1600 : (40 * P + 32) * 3840 + J * 80;
      dmam<60>(rs_2, v2, s2, T4 == 1 ? L_WPHI : L_WR + 7680); dmam<60>(rs_2, v2, s2 + (T4 == 1 ? 640 : 4 * 3840), T4 == 1 ? L_WPHI + 640 : L_WR + 8640);
    }
  {
    const u32 cm = (p4 >> 1) * 40 + (p4 & 1) * 16;
    ta[0] = opq(L_WR + q4 * 240 + 80 + hh * 80 + cm + g2 * 8);
    ta[1] = opq(L_WR + q4 * 240 + (g4 == 0 ? 80 + (p4 & 1) * 80 + (p4 >> 1) * 40 + 32 : g4 == 3 ? (p4 & 1) * 40 + 32 : cm + (g4 - 1) * 8));
    const u32 sA = g4 == 0 ? 5120 : 2560, sB = g4 == 3 ? 128 : 256, sC = (g4 == 0 ? 4096 : (g4 == 3 && t16 >= 8) ? OOB : 102400 + (g4 - 1) * 1024) + t16 * 16;
#pragma unroll
    for (int z = 0; z < 2; ++z) {
      const int oct = 5 * P + (z ? 4 : T4);
      td[z][0] = opq(wqs + (oct >> 2) * 5120 + (oct & 3) * 256 + g4 * 1024 + t16 * 16);
      td[z][1] = opq(wqs + (oct >> 2) * sA + (oct & 3) * sB + sC);
    }
  }
  }
  u32 tag;
  bar();
  const u32 xsb = wave * 12288;
  u32 vxs[2];
#pragma unroll
  for (int z = 0; z < 2; ++z) vxs[z] = (8 * z + (lane >> 3)) * 1280 + (((lane & 7) ^ (4 * z + (lane >> 4))) << 4);
  auto x1p = [&](int p) { dma16(rs_x, vxs[p & 1], (128 * P + 16 * wave) * 1280 + wrap((p >> 1) + R10, 10) * 128, xsb + (p % 12) * 1024); };
  sfor<0, 12>([&](auto I) { x1p(I.v); });
  await1<18>(cq);
  float sv[5];
  {
    const u32 sh = (coff2 & 2) * 8;
    const u32 ce[3] = {__builtin_amdgcn_alignbit(cq[1], cq[0], sh), __builtin_amdgcn_alignbit(cq[2], cq[1], sh), cq[2] >> sh};
#pragma unroll
    for (int i = 0; i < 5; ++i) {
      const float cv = (i & 1) ? bfhi(ce[i >> 1]) : bflo(ce[i >> 1]);
      sv[i] = rbf(cv * __builtin_amdgcn_rcpf(1.f + __builtin_amdgcn_exp2f(-1.4426950408889634f * cv)));
      asm volatile("" : "+v"(sv[i]));
    }
  }
  await1<12>(tagq);
  asm volatile("" : "+v"(wm[0]), "+v"(wm[1]), "+v"(wm[2]), "+v"(wm[3]), "+v"(wm[4]));
  tag = tagq[0] + 1u;
  WST(0);
  if (!K2) {
    float acc[8] = {0.f, 0.f, 0.f, 0.f, 0.f, 0.f, 0.f, 0.f};
#pragma unroll
    for (int i = 0; i < 5; ++i)
#pragma unroll
      for (int e = 0; e < 4; ++e) { acc[2 * e] += sv[i] * bflo(wm[i][e]); acc[2 * e + 1] += sv[i] * bfhi(wm[i][e]); }
    ldswf(L_KVB + T4 * 256 + ((lane & 7) * 8 + 4 * hh + 2 * g2 + ((lane >> 3) & 1)) * 4, red8(acc, lane));
    if (wave) lds_post(L_SYNC + 12 + wave * 4, 1u);
    else {
      lds_wait(L_SYNC + 16 + (lane % 3) * 4, 1u);
      const u32 a = L_KVB + lane * 4;
      const float v = (ldsrf(a) + ldsrf(a + 256)) + (ldsrf(a + 512) + ldsrf(a + 768));
      if (mc < 20) {
        __builtin_amdgcn_raw_buffer_store_b32(__float_as_uint(v), rs_ws, WS_ST1 + (mk * 1280 + mc * 64 + lane) * 4, 0, AUX_SC1);
        drain();
        if (lane < 16) setflag(ws, WS_FLAG + (6 * 16 + lane) * 512 + b * 4, tag);
      } else stb64c(rs_ws, b < 240 ? WS_MODG + (mk * 3840 + mc * 64 + lane) * 8 : OOB, v2u{__float_as_uint(v), tag});
    }
  } else {
    const u32 ro = T4 * 1920;
    const v8bf f0 = trfrag(ta[0] + ro, ta[0] + ro + 960), f1 = trfrag(ta[1] + ro, ta[1] + ro + 960);
    const u32 tz = (T4 >> 1) ? ta[T4 & 1] : ta[0];
    const v8bf f2 = trfrag(tz + 7680, tz + 8640);
    stb128c(rs_ws, td[0][0], __builtin_bit_cast(v4u, f0)); stb128c(rs_ws, td[0][1], __builtin_bit_cast(v4u, f1));
    stb128c(rs_ws, (T4 >> 1) ? ((T4 & 1) ? td[1][1] : td[1][0]) : OOB, __builtin_bit_cast(v4u, f2));
    drain();
    if (lane == 0) setflag(ws, WS_FLAG + (5 * 16 + J) * 512 + (P * 4 + T4) * 4, tag);
  }
  WST(1);
  const rsrc_t rs_r = mkrs(wave < 2 ? wg : wave < 4 ? wu : wave < 6 ? wd : wo, wave < 6 ? DM * FF * 2 : DM * DM * 2);
  u32 v_r, so_r[2], ld_r;
  const u32 mk_r = K2 ? (wave < 6 ? 0xfffffffu : 0x3ffffu) : 0xffffffu;
  {
    const int row = lane / 14; int ch = lane - row * 14; if (ch >= 2 * nt) ch -= 2;
    v_r = opq(K2 ? v10 : row * 3456 + ch * 16);
#pragma unroll
    for (int z = 0; z < 2; ++z) {
      const int o = (rq27 < 27 ? 8 * rq27 : 0) + 4 * (wave & 1) + 2 * z;
      int jj, oo;
      if (o < 128) { jj = o >> 3; oo = o & 7; } else if (o < 192) { jj = (o - 128) >> 2; oo = 8 + (o & 3); } else { const int hq = (o - 192) >> 1; jj = hq + hq / 3; oo = 12; }
      const int wdrow = (7 * jj - (jj >> 2)) * 16 + oo * 8;
      so_r[z] = sopq(!K2 ? (40 * P + 20 * (wave & 1) + 8 * z) * 3456 + t0 * 32 : (wave < 6 ? wdrow : 40 * P + 20 * (wave & 1) + 5 * z) * 1280 + CT * 160);
    }
    ld_r = sopq(!K2 ? L_R2 + wave * 4480 : wave < 6 ? L_RD + (wave & 1) * 5120 : L_OR + (wave & 1) * 3200);
  }
  auto raw1 = [&](int i) {
    const int z = i / 3, k = i % 3;
    const u32 so = so_r[z] + (K2 ? 5 * k * 1280 : 4 * k * 3456), ld = ld_r + (!K2 ? (2 * z + k) * 896 : wave < 6 ? z * 2560 + k * 800 : (z + k) * 800);
    asm volatile("s_mov_b32 m0, %0\n\ts_mov_b32 exec_hi, %4\n\tbuffer_load_dwordx4 %1, %2, %3 offen nt lds\n\ts_mov_b32 exec_hi, -1" ::"s"(ld), "v"(v_r), "s"(rs_r), "s"(so), "s"(mk_r) : "memory");
  };
  u32 ts0[5], ts1[5], tdd[5];
  auto tileaddr = [&]() {
#pragma unroll
  for (int z = 0; z < 5; ++z) {
    const int k = wave + 8 * z;
    u32 src, rst = 160, dst;
    if (k < 18) {
      const bool blk = k < 15;
      const int o5b = k / 3, bk = k - o5b * 3, o5 = blk ? o5b : 2 * (k - 15) + hh, oct = 5 * P + o5;
      src = L_R2 + (o5 < 5 ? o5 : 0) * 1792 + (blk ? hh * 8960 + bk * 64 + p4 * 16 + g2 * 8 : g2 * 8960 + 192 + p4 * 8); rst = 224;
      dst = WS_GU + J * 286720 + (oct & 3) * 256 + t16 * 16 + (blk ? (bk < 2 ? (oct >> 2) * 8192 + bk * 4096 : 163840 + (oct >> 2) * 6144) + g4 * 1024 : 163840 + (oct >> 2) * 6144 + 4096 + g2 * 1024);
      dst = o5 < 5 ? dst : OOB;
    } else if (k < 28) {
      const int kk = k - 18, og = kk >= 5, tl = kk - 5 * og;
      src = L_RD + (4 * og + g4) * 1280 + tl * 32 + p4 * 8;
      dst = rq27 < 27 ? WS_WD + CT * 276480 + (2 * rq27 + og) * 5120 + tl * 1024 + vlin : OOB;
    } else {
      const bool mn = k < 33;
      const int o5 = mn ? k - 28 : 4 * (k - 33) + g4, tl = mn ? g4 : 4, oct = 5 * P + o5;
      src = L_OR + (o5 < 5 ? o5 : 0) * 1280 + tl * 32 + p4 * 8;
      dst = o5 < 5 ? WS_WO + J * 102400 + (oct >> 2) * 5120 + tl * 1024 + (oct & 3) * 256 + t16 * 16 : OOB;
    }
    ts0[z] = opq(src + q4 * rst); ts1[z] = opq(src + (4 + q4) * rst); tdd[z] = opq(dst);
  }
  };
  v4u hf[20];
  {
    u32 ra[2];
#pragma unroll
    for (int q = 0; q < 2; ++q) ra[q] = opq(xsb + t16 * 128 + (((4 * q + g4) ^ (t16 >> 1)) << 4));
    drain();
#pragma unroll
    for (int n = 0; n < 8; ++n) hf[n] = ldsr128(ra[n & 1] + (n >> 1) * 2048);
    asm volatile("s_waitcnt lgkmcnt(0)" : "+v"(hf[0]), "+v"(hf[1]), "+v"(hf[2]), "+v"(hf[3]), "+v"(hf[4]), "+v"(hf[5]), "+v"(hf[6]), "+v"(hf[7])::"memory");
    sfor<12, 20>([&](auto I) { x1p(I.v); });
#pragma unroll
    for (int n = 8; n < 12; ++n) hf[n] = ldsr128(ra[n & 1] + (n >> 1) * 2048);
    SB();
    tileaddr();
    drain();
#pragma unroll
    for (int n = 12; n < 20; ++n) hf[n] = ldsr128(ra[n & 1] + ((n - 12) >> 1) * 2048);
  }
  float rs1, mr1;
  {
    v4f d1 = {}, d2 = {};
    const v4u ones = {0x3f803f80u, 0x3f803f80u, 0x3f803f80u, 0x3f803f80u};
#pragma unroll
    for (int n = 0; n < 20; ++n) { d1 = mfma16(asbf(ones), asbf(hf[n]), d1); d2 = mfma16(asbf(hf[n]), asbf(hf[n]), d2); }
    float dg = p4 == 0 ? d2[0] : p4 == 1 ? d2[1] : p4 == 2 ? d2[2] : d2[3];
    dg = q4 == g4 ? dg : 0.f;
    const float m = d1[0] * (1.f / DM);
    const float var = fmaxf(xsum32(xsum16(dg)) * (1.f / DM) - m * m, 0.f);
    rs1 = __builtin_amdgcn_rsqf(var + 1e-5f); mr1 = -m * rs1;
    asm volatile("" : "+v"(rs1), "+v"(mr1));
  }
  WST(3);
  if (wave < 4) {
    poll_n(ws, WS_FLAG + (6 * 16 + P) * 512 + wave * 80, 20, tag, lane);
    v4u g[2][4];
#pragma unroll
    for (int z = 0; z < 2; ++z)
#pragma unroll
      for (int k = 0; k < 4; ++k) g[z][k] = ldb128c(rs_ws, (80 * wave + (z ? 64 + t16 : lane)) * 16, WS_ST1 + k * 5120);
    const float one = (wave >> 1) ? 1.f : 0.f;
#pragma unroll
    for (int z = 0; z < 2; ++z) {
      const v4f sm = (__builtin_bit_cast(v4f, g[z][0]) + __builtin_bit_cast(v4f, g[z][1])) + (__builtin_bit_cast(v4f, g[z][2]) + __builtin_bit_cast(v4f, g[z][3]));
      ldsw128f(((wave >> 1) ? L_COA : L_COS) + wrap(80 * (wave & 1) + (z ? 64 + t16 : lane) + 160 - 16 * R10, 160) * 16,
               v4f{rbf(one + rbf(sm[0])), rbf(one + rbf(sm[1])), rbf(one + rbf(sm[2])), rbf(one + rbf(sm[3]))});
    }
  } else if (wave == 5) poll_n(ws, WS_FLAG + (5 * 16 + J) * 512, 64, tag, lane);
  WST(4);
  bar();
  STAMP(1);
  auto kv1 = [&](int z) { const int e = z < 12 ? 8 * z + wave : 96 + T4; dma16c(rs_ws, vlin, wqs + wrap(e + 10 * R10, 100) * 1024, e * 1024); };
  kv1(0); kv1(1); kv1(2);
  {
    const u32 cb = opq(L_COA + g4 * 32);
    v4f cA[4], cS[4];
#pragma unroll
    for (int i = 0; i < 4; ++i) { cA[i] = ldsr128f(cb + (i >> 1) * 128 + (i & 1) * 16); cS[i] = ldsr128f(cb + 2560 + (i >> 1) * 128 + (i & 1) * 16); }
    SB();
#pragma unroll
    for (int i = 0; i < 40; ++i) {
      const int n = i >> 1, z = i & 1;
      const u32 a = hf[n][2 * z], c = hf[n][2 * z + 1];
      const v4f y = (v4f{bflo(a), bfhi(a), bflo(c), bfhi(c)} * rs1 + mr1) * cA[i & 3] + cS[i & 3];
      hf[n][2 * z] = pkbf(y[0], y[1]); hf[n][2 * z + 1] = pkbf(y[2], y[3]);
      if (i + 4 < 40) { cA[i & 3] = ldsr128f(cb + (n + 2) * 128 + z * 16); cS[i & 3] = ldsr128f(cb + 2560 + (n + 2) * 128 + z * 16); }
      if (z) asm volatile("" : "+v"(hf[n]));
      if (z && n < 10) kv1(3 + n);
      if (i == 33) { drain(); rawbar(); }
      if (i >= 34) raw1(i - 34);
      SB();
    }
  }
  WST(5);
  v4u wpf[5][2];
  {
    const u32 mk0 = (q4 >> 1) == hh ? 0xffffffffu : 0u, mk1 = g2 ? 0u : mk0, sgn = (q4 & 1) ? 0x80008000u : 0u;
    const u32 r0 = L_WPHI + hh * 800 + q4 * 40;
#pragma unroll
    for (int T = 0; T < 5; ++T) {
      const u32 c = ((p4 & 1) ? (T + 1) % 5 : T) * 8;
      wpf[T][0] = __builtin_bit_cast(v4u, trfrag(r0 + g2 * 320 + c, r0 + g2 * 320 + 160 + c));
      const v2u l = __builtin_bit_cast(v2u, trd(r0 + 640 + c));
#pragma unroll
      for (int e = 0; e < 4; ++e) wpf[T][0][e] = (wpf[T][0][e] ^ sgn) & mk0;
      wpf[T][1] = v4u{(l[0] ^ sgn) & mk1, (l[1] ^ sgn) & mk1, 0u, 0u};
      asm volatile("" : "+v"(wpf[T][0]), "+v"(wpf[T][1]));
    }
  }
  WST(6);
  WST(2);
  STAMP(2);
  STAMP(3);
  u32 lb[3];
  lb[0] = opq(vlin); lb[1] = opq(vlin + 51200); lb[2] = opq(vlin + 102400);
  auto frag = [&](u32 off) { return asbf(ldsr128(lb[off / 51200] + off % 51200)); };
  v4f aC[5] = {};
  {
    v8bf wf[10], tfr;
    sfor<0, 10>([&](auto F) { wf[F.v] = frag(F.v * 1024); });
    SB();
    sfor<0, 20>([&](auto N) {
      constexpr int n = N.v;
      if constexpr (n == 12) { waitv<2>(); rawbar(); SB(); }
      sfor<0, 5>([&](auto T) {
        constexpr int t = T.v, f = (5 * n + t) % 10;
        aC[t] = mfma16(wf[f], asbf(hf[n]), aC[t]);
        if constexpr (n < 18) wf[f] = frag((5 * n + 10 + t) * 1024);
        if constexpr (t == 2 && n >= 13 && n < 18) stb128c(rs_ws, tdd[n >= 13 && n < 18 ? n - 13 : 0], __builtin_bit_cast(v4u, tfr));
        if constexpr (t == 3 && n >= 12 && n < 17) tfr = trfrag(ts0[n >= 12 && n < 17 ? n - 12 : 0], ts1[n >= 12 && n < 17 ? n - 12 : 0]);
        if constexpr (t == 0 && n < 2) {
          constexpr int z = n;
          const int e = z < 6 ? 8 * z + wave : 48 + (wave & 1);
          dma16c(rs_ws, vlin, wqs + 102400 + wrap(e + 5 * R10, 50) * 1024, L_RQ + e * 1024);
        }
        SB();
      });
    });
    asm volatile("" : "+v"(aC[0]), "+v"(aC[1]), "+v"(aC[2]), "+v"(aC[3]), "+v"(aC[4]));
  }
  WST(7);
  STAMP(4);
  auto phi = [&](const v4f& m0, const v4f& m1, const v4f& l, u32 (&o)[10]) {
    const v4u b0 = {pkbf(m0[0], m0[1]), pkbf(m0[2], m0[3]), pkbf(m1[0], m1[1]), pkbf(m1[2], m1[3])};
    const v4u b1 = {pkbf(l[0], l[1]), pkbf(l[2], l[3]), 0u, 0u};
    float pv[20];
#pragma unroll
    for (int T = 0; T < 5; ++T) {
      v4f d = {};
      d = mfma16(asbf(wpf[T][0]), asbf(b0), d); d = mfma16(asbf(wpf[T][1]), asbf(b1), d);
#pragma unroll
      for (int e = 0; e < 4; ++e) pv[4 * T + e] = d[e];
    }
    softmax20(pv, o);
  };
  {
    u32 pk[10];
    phi(aC[0], aC[1], aC[4], pk);
    const int tok = 16 * wave + t16;
    const u32 fx = (((tok >> 1) & 1) << 2) | ((tok & 1) << 1) | ((tok >> 2) & 1), rb = hh * 16384 + tok * 128;
    auto cad = [&](int cc) { return rb + ((cc ^ fx) << 4); };
    drain();
    bar();
    if (K2) {
#pragma unroll 1
      for (int e = 16 + T4; e < 50; e += 4) dma16c(rs_ws, vlin, wqs + 102400 + wrap(e + 5 * R10, 50) * 1024, L_RQ + e * 1024);
    }
    ldsw128(cad(g2), v4u{pkbf(aC[2][0], aC[2][1]), pkbf(aC[2][2], aC[2][3]), pkbf(aC[3][0], aC[3][1]), pkbf(aC[3][2], aC[3][3])});
    if (g2) ldsw128(cad(2), v4u{pkbf(aC[4][0], aC[4][1]), pkbf(aC[4][2], aC[4][3]), 0x00003f80u, 0u});
    ldsw128(cad(g2 ? 7 : 4), v4u{pk[0], pk[1], pk[2], pk[3]});
    ldsw128(cad(g2 ? 3 : 5), v4u{pk[4], pk[5], pk[6], pk[7]});
    ldsw64(cad(6) + g2 * 8, v2u{pk[8], pk[9]});
  }
  bar();
  STAMP(5);
  if (wave < 4) {
    const int hd = wave >> 1, tl = wave & 1;
    u32 av[2], ap[2];
#pragma unroll
    for (int t = 0; t < 2; ++t) {
      const int tok = 8 * hh + 4 * t + q4;
      const u32 fx = (((tok >> 1) & 1) << 2) | ((tok & 1) << 1) | ((tok >> 2) & 1), cl = 2 * g2 + (p4 >> 1);
      av[t] = hd * 16384 + tok * 128 + ((cl ^ fx) << 4) + 8 * (p4 & 1);
      ap[t] = hd * 16384 + tok * 128 + ((((tl ? 0 : 4) + cl) ^ fx) << 4) + 8 * (p4 & 1);
    }
    v8bf fv[8], fp[8];
#pragma unroll
    for (int s = 0; s < 8; ++s) { fp[s] = trfrag(ap[0] + s * 2048, ap[1] + s * 2048); fv[s] = trfrag(av[0] + s * 2048, av[1] + s * 2048); }
    v16f d = {};
#pragma unroll
    for (int s = 0; s < 8; ++s) d = mfma32(fp[s], fv[s], d);
    const u32 base = i32 < 21 ? WS_KVP + (J * 16 + P) * KVP_STRIDE + hd * 1680 + i32 * 8 + hh * 168 : OOB;
    if (tl == 0) {
#pragma unroll
      for (int rq = 0; rq < 3; ++rq) stb64c(rs_ws, base + rq * 336, v2u{pkbf(d[4 * rq], d[4 * rq + 1]), pkbf(d[4 * rq + 2], d[4 * rq + 3])});
    }
    stb64c(rs_ws, base + (3 + tl) * 336, v2u{pkbf(d[12], d[13]), pkbf(d[14], d[15])});
    drain();
    if (lane == 0) setflag(ws, WS_FLAG + (0 * 16 + J) * 512 + (P * 4 + wave) * 4, tag);
    STAMP(6);
  }
  if (K2) { drain(); lds_post(L_SYNC + 32 + wave * 4, 1u); }
  lds_wait(L_SYNC + 48 + (lane & 3) * 4, 1u);
  WST(8);
  const int bq = sopq(b);
  const bool dog2 = bq < 216;
  const int g2c = bq >> 2, g2k = bq & 3;
  v4u gw[5], gq[4], gr[4];
  const int ge = lane < 40 ? lane : 39, gvec = ge >= 20;
  const u32 gof = K2 ? OOB : WS_MODG + ((wave == 1 ? 1280 : wave == 2 ? 3200 : 2560) + CT * 80 + 2 * ge) * 8;
  const u32 gor = K2 ? OOB : WS_MODG + (1920 + gvec * 640 + 160 * g2k + 40 * T4) * 8 + (ge - 20 * gvec) * 16;
  if (K2) {
    const u32 so = (128 * P + 64 * TH + 16 * T4) * 1280 + CT * 160;
#pragma unroll
    for (int k = 0; k < 3; ++k) dmam<60>(rs_x, v10, so + 5 * k * 1280, 32768 + T4 * 2560 + k * 800);
  } else {
    const rsrc_t rs_gu = mkrs(g2c < 27 ? wg : wu, DM * FF * 2);
    const u32 vo = dog2 ? (160 * g2k + 40 * T4 + (lane >> 3)) * 3456 + (g2c < 27 ? g2c : g2c - 27) * 128 + (lane & 7) * 16 : OOB;
#pragma unroll
    for (int i = 0; i < 5; ++i) gw[i] = ldb128(rs_gu, vo, i * 8 * 3456);
#pragma unroll
    for (int i = 0; i < 4; ++i) { gr[i] = ldb128c(rs_ws, gor, i * 30720); gq[i] = ldb128c(rs_ws, gof, i * 30720); }
  }
  SB();
  auto wo1 = [&](int e) { dma16c(rs_ws, vlin, WS_WO + J * 102400 + e * 1024, e * 1024); };
  u32 pq[10];
  {
    v4f aQ[3] = {};
    const u32 bl = opq(L_RQ + 2048 + g4 * 128 + ((t16 >> 3) * 4 + p4) * 16);
    auto qf = [&](int n, int t) { return asbf(ldsr128(t == 2 ? bl + n * 2560 : lb[2] + n * 2560 + t * 1024)); };
    v8bf wf[9];
#pragma unroll
    for (int f = 0; f < 9; ++f) wf[f] = qf(f / 3, f % 3);
    SB();
#pragma unroll
    for (int n = 0; n < 60; ++n) {
      aQ[n % 3] = mfma16(wf[n % 9], asbf(hf[n / 3]), aQ[n % 3]);
      if (n + 9 < 60) wf[n % 9] = qf((n + 9) / 3, n % 3);
      SB();
    }
    asm volatile("" : "+v"(aQ[0]), "+v"(aQ[1]), "+v"(aQ[2]));
    phi(aQ[0], aQ[1], aQ[2], pq);
#pragma unroll
    for (int i = 0; i < 10; ++i) asm volatile("" : "+v"(pq[i]));
  }
  WST(9);
  v2u xq[5];
  if (wave < 4) {
    if (!tags4(gr[0], gr[1], gr[2], gr[3], tag)) gather4(rs_ws, gor, 30720, tag, gr[0], gr[1], gr[2], gr[3]);
    if (!tags4(gq[0], gq[1], gq[2], gq[3], tag)) gather4(rs_ws, gof, 30720, tag, gq[0], gq[1], gq[2], gq[3]);
    *(LAS v2f*)(size_t)(L_G2A + T4 * 320 + ge * 8) = sum4(gr[0], gr[1], gr[2], gr[3], gvec ? 1.f : 0.f);
    if (wave) *(LAS v2f*)(size_t)(L_WPHI + (wave - 1) * 320 + ge * 8) = sum4(gq[0], gq[1], gq[2], gq[3], wave == 3 ? 1.f : 0.f);
    float sa[8] = {}, sb2[8] = {};
#pragma unroll
    for (int i = 0; i < 5; ++i) {
      const float cs = ldsrf(L_G2A + T4 * 320 + (8 * i + (lane >> 3)) * 4), ca = ldsrf(L_G2A + T4 * 320 + 160 + (8 * i + (lane >> 3)) * 4);
#pragma unroll
      for (int z = 0; z < 4; ++z) {
        const float lo = bflo(gw[i][z]), hi = bfhi(gw[i][z]);
        sa[2 * z] += ca * lo; sa[2 * z + 1] += ca * hi; sb2[2 * z] += cs * lo; sb2[2 * z + 1] += cs * hi;
      }
    }
    const u32 a = L_G2R + T4 * 512 + ((lane & 7) * 8 + 4 * hh + 2 * g2 + ((lane >> 3) & 1)) * 4;
    ldswf(a, red8(sa, lane)); ldswf(a + 256, red8(sb2, lane));
    lds_wait(L_SYNC + 48 + (lane & 3) * 4, 2u);
#pragma unroll 1
    for (int e = wave; e < WO_EARLY; e += 4) wo1(e);
  } else {
    drain();
#pragma unroll
    for (int t = 0; t < 5; ++t) { xq[t] = ldsr64(32768 + T4 * 2560 + t16 * 160 + t * 32 + g4 * 8); asm volatile("" : "+v"(xq[t])); }
    if (wave == 4) { poll_n(ws, WS_FLAG + (0 * 16 + J) * 512, 64, tag, lane); lds_post(L_SYNC + 28, 1u); } else lds_wait(L_SYNC + 28, 1u);
    const int t2 = tid - 256;
    v4u pv[16];
    const u32 c16 = t2 < 210 ? t2 * 16 : OOB;
#pragma unroll
    for (int i = 0; i < 16; ++i) pv[i] = ldb128c(rs_ws, c16, WS_KVP + (J * 16 + i) * KVP_STRIDE);
    lds_post(L_SYNC + 32 + wave * 4, 2u);
    u32 la[2];
#pragma unroll
    for (int z = 0; z < 2; ++z) { const int c = t2 < 210 ? 2 * t2 + z : 0, hd = c >= 210, rem = c - hd * 210, fq = rem / 21; la[z] = opq(L_KVB + hd * 1680 + (rem - fq * 21) * 80 + fq * 8); }
    SB();
    float sm[8] = {};
#pragma unroll
    for (int i = 0; i < 16; ++i)
#pragma unroll
      for (int e = 0; e < 4; ++e) { sm[2 * e] += bflo(pv[i][e]); sm[2 * e + 1] += bfhi(pv[i][e]); }
    if (t2 < 210) { ldsw64(la[0], v2u{pkbf(sm[0], sm[1]), pkbf(sm[2], sm[3])}); ldsw64(la[1], v2u{pkbf(sm[4], sm[5]), pkbf(sm[6], sm[7])}); }
  }
  STAMP(7); STAMP(8);
  WST(10);
  bar();
  STAMP(9);
  {
    const bool act = (t16 >> 3) == hh;
    const int hf2 = q4 & 1;
    const u32 kb = L_KVB + hh * 1680;
    const u32 a01 = kb + (4 * hh + 8 * hf2 + p4) * 80, a2 = kb + (hf2 ? 20 : (hh ? 0 : 16) + p4) * 80;
    v4f d[3] = {};
    const v4u b0 = {pq[0], pq[1], pq[2], pq[3]}, b1 = {pq[4], pq[5], pq[6], pq[7]}, b2 = {pq[8], pq[9], 0u, 0u};
#pragma unroll
    for (int T = 0; T < 3; ++T) {
      const u32 r = T < 2 ? a01 + T * 320 : a2;
      const v4u k0 = ldsr128(act ? r + g2 * 48 : L_ZERO), k1 = ldsr128(act ? r + 16 + g2 * 48 : L_ZERO);
      const v2u k2 = ldsr64(act ? r + 32 + g2 * 8 : L_ZERO);
      d[T] = mfma16(asbf(k0), asbf(b0), d[T]); d[T] = mfma16(asbf(k1), asbf(b1), d[T]); d[T] = mfma16(asbf(v4u{k2[0], k2[1], 0u, 0u}), asbf(b2), d[T]);
    }
    const auto dn = __builtin_amdgcn_permlane16_swap(__float_as_uint(d[2][0]), __float_as_uint(d[2][0]), false, false);
    const float rd = 1.0f / (__uint_as_float(dn[1]) + 1e-6f);
    const u32 ob = WS_O + ((8 * P + wave) * 80 + 5 * J) * 256 + t16 * 16;
    stb128c(rs_ws, ob + (3 * hh + g2) * 256, v4u{pkbf(d[0][0] * rd, d[0][1] * rd), pkbf(d[0][2] * rd, d[0][3] * rd), pkbf(d[1][0] * rd, d[1][1] * rd), pkbf(d[1][2] * rd, d[1][3] * rd)});
    stb64c(rs_ws, g2 ? OOB : ob + 512 + hh * 8, v2u{pkbf(d[2][0] * rd, d[2][1] * rd), pkbf(d[2][2] * rd, d[2][3] * rd)});
  }
  publish(ws, WS_FLAG + (1 * 16 + P) * 512 + J * 4, tag);
  STAMP(10);
  R20 = sopq(R20);
  const u32 gus = WS_GU + J * 286720;
  const int tokE = 128 * P + 64 * TH + 16 * T4 + t16;
  if (dog2 && wave == 7) {
    const u32 a = L_G2R + lane * 4;
    const float av = (ldsrf(a) + ldsrf(a + 512)) + (ldsrf(a + 1024) + ldsrf(a + 1536));
    const float bv = (ldsrf(a + 256) + ldsrf(a + 768)) + (ldsrf(a + 1280) + ldsrf(a + 1792));
    stb64c(rs_ws, WS_AB + (g2k * 3456 + g2c * 64 + lane) * 16, v2u{__float_as_uint(av), tag});
    stb64c(rs_ws, WS_AB + (g2k * 3456 + g2c * 64 + lane) * 16 + 8, v2u{__float_as_uint(bv), tag});
  }
  if (wave >= 1) {
#pragma unroll 1
    for (int e = WO_EARLY + wave - 1; e < 100; e += 7) wo1(e);
    drain();
  }
  SB();
  WST(11);
  if (wave == 0) poll_n(ws, WS_FLAG + (1 * 16 + P) * 512, 16, tag, lane);
  bar();
  STAMP(11);
  auto gu1 = [&](int e8) {
    const u32 src = e8 < 160 ? wrap(e8 + 8 * R20, 160) + wave : 160 + wrap(e8 - 160 + wave + 6 * R20, 120);
    dma16c(rs_ws, vlin, gus + src * 1024, ((e8 + 104) % 144 + wave) * 1024);
  };
  u32 kb2[3];
  kb2[0] = opq(vlin + K2 * 5120); kb2[1] = opq(vlin + K2 * 5120 + 51200); kb2[2] = opq(vlin + K2 * 5120 + 102400);
  auto frag2 = [&](u32 off) { return asbf(ldsr128(kb2[off / 51200] + off % 51200)); };
  float x1[20];
  {
    v4u of[10];
    const u32 ab = WS_O + ((8 * P + 4 * TH + T4) * 80 + 4 * K2) * 256 + vlin;
    sfor<0, 10>([&](auto S) { aloadc<(S.v & 1) * 2048>(of[S.v], rs_ws, ab, (S.v >> 1) * 4096); });
    sfor<0, 5>([&](auto I) { gu1(8 * I.v); });
    v8bf wf[10];
    sfor<0, 10>([&](auto F) { wf[F.v] = frag2((F.v / 5) * 10240 + (F.v % 5) * 1024); });
    v4f acc[5] = {};
    SB();
    sfor<0, 10>([&](auto S) {
      constexpr int s = S.v;
      await1<14 - s>(of[s]);
      sfor<0, 5>([&](auto T) {
        constexpr int t = T.v, f = (5 * s + t) % 10;
        acc[t] = mfma16(wf[f], asbf(of[s]), acc[t]);
        if constexpr (s < 8) wf[f] = frag2((s + 2) * 10240 + t * 1024);
        SB();
      });
    });
    asm volatile("" : "+v"(acc[0]), "+v"(acc[1]), "+v"(acc[2]), "+v"(acc[3]), "+v"(acc[4]));
    WST(12);
    bar();
    const u32 xb = 81920 + T4 * 5120 + vlin;
    if (K2 == 0) {
#pragma unroll
      for (int t = 0; t < 5; ++t) ldsw128f(xb + t * 1024, acc[t]);
    }
    v4f ga[5], A2[5];
#pragma unroll
    for (int t = 0; t < 5; ++t) { ga[t] = ldsr128f(L_WPHI + t * 64 + g4 * 16); A2[t] = ldsr128f(L_WPHI + 640 + t * 64 + g4 * 16); asm volatile("" : "+v"(ga[t]), "+v"(A2[t])); }
    const u32 uo = opq(WS_U + ((tokE >> 4) * 80 + CT * 10 + hh) * 256 + t16 * 16 + g2 * 8);
    bar();
    if (K2 == 1) {
      v4f s1 = {}, s2 = {};
#pragma unroll
      for (int t = 0; t < 5; ++t) {
        const v4f y = v4f{bflo(xq[t][0]), bfhi(xq[t][0]), bflo(xq[t][1]), bfhi(xq[t][1])} + ga[t] * (acc[t] + ldsr128f(xb + t * 1024));
        const u32 p0 = pkbf(y[0], y[1]), p1 = pkbf(y[2], y[3]);
        const v4f r = {bflo(p0), bfhi(p0), bflo(p1), bfhi(p1)}, u = r * A2[t];
        stb64c(rs_ws, uo + t * 512, v2u{pkbf(u[0], u[1]), pkbf(u[2], u[3])});
        x1[4 * t] = r[0]; x1[4 * t + 1] = r[1]; x1[4 * t + 2] = r[2]; x1[4 * t + 3] = r[3];
        s1 += r;
      }
      const float mt = xsum32(xsum16((s1[0] + s1[1]) + (s1[2] + s1[3]))) * (1.f / 80.f);
#pragma unroll
      for (int t = 0; t < 5; ++t) { const v4f d = v4f{x1[4 * t], x1[4 * t + 1], x1[4 * t + 2], x1[4 * t + 3]} - mt; s2 += d * d; }
      const float m2 = xsum32(xsum16((s2[0] + s2[1]) + (s2[2] + s2[3])));
      stb64c(rs_ws, g4 == 0 ? WS_STAT + ((P * 8 + CT) * 128 + (tokE & 127)) * 8 : OOB, v2u{__float_as_uint(mt), __float_as_uint(m2)});
      drain();
      if (lane == 0) setflag(ws, WS_FLAG + (2 * 16 + P) * 512 + (J * 4 + T4) * 4, tag);
#pragma unroll 1
      for (int z = 0; z < 6; ++z) { const int e = z < 5 ? 120 + 5 * T4 + z : 140 + T4; dma16c(rs_ws, vlin, gus + wrap(e + 8 * R20, 160) * 1024, (e - 40) * 1024); }
    } else {
#pragma unroll
      for (int i = 0; i < 20; ++i) x1[i] = 0.f;
      {
        const int abe = T4 * 56 + (lane < 56 ? lane : 55), mat = abe >= 112, c = abe - 112 * mat;
        v4u g0, g1, g2v, g3;
        gather4(rs_ws, WS_AB + (mat * 1728 + (t0 * 16 + c < 1728 ? t0 * 16 + c : 1727)) * 16, 3456 * 16, tag, g0, g1, g2v, g3);
        ldswf(L_AB + abe * 4, (__uint_as_float(g0[0]) + __uint_as_float(g1[0])) + (__uint_as_float(g2v[0]) + __uint_as_float(g3[0])));
        ldswf(L_AB + 896 + abe * 4, (__uint_as_float(g0[2]) + __uint_as_float(g1[2])) + (__uint_as_float(g2v[2]) + __uint_as_float(g3[2])));
      }
      if (wave > 0) {
#pragma unroll 1
        for (int e = 39 + wave; e < 120; e += 3) { dma16c(rs_ws, vlin, gus + wrap(e + 8 * R20, 160) * 1024, (e - 40) * 1024); waitv<GU_THR>(); }
      }
    }
  }
  STAMP(12);
  WST(13);
  const u32 wds = WS_WD + CT * 276480;
  v4u af[27];
  const u32 ab4 = WS_ACT + ((8 * P + 4 * TH + T4) * 216 + 4 * K2) * 256 + vlin;
  auto lda = [&](auto N) { constexpr int n = N.v; aloadc<0>(af[n], rs_ws, ab4, (n < 16 ? wrap(n + R4A, 16) : 16 + wrap(n - 16 + R4B, 11)) * 2048); };
  auto wflag = [&](int hop) { bflag(rs_ws, tag, WS_FLAG + (hop * 16 + P) * 512 + (wave * 16 + J) * 4); };
  auto wd1 = [&](int e) { dma16c(rs_ws, vlin, wds + (e < 160 ? wrap(e + 10 * R4A, 160) : 160 + wrap(e - 160 + 10 * R4B, 110)) * 1024, wrap(wrap(e + 100, 150), 150) * 1024); };
  {
    if (wave == 0) poll_n(ws, WS_FLAG + (2 * 16 + P) * 512, 64, tag, lane);
    bar();
    STAMP(13);
    R20 = sopq(R20);
    v4u uf[20], st;
    const u32 uvo = WS_U + (8 * P + wave) * 20480 + vlin;
    constexpr int U0 = 8;
    auto ldu = [&](auto N) { aloadc<0>(uf[N.v], rs_ws, uvo, wrap(N.v + R20, 20) * 1024); };
    sfor<0, U0>(ldu);
    const u32 stoff = WS_STAT + ((P * 8 + 2 * g4 + (lane & 1)) * 128 + 16 * wave + (t16 & ~1)) * 8;
    constexpr auto sl = [](int e) { return (u32)(((e + 104) % 144) * 1024); };
    v8bf wf[8];
    sfor<0, 8>([&](auto F) { wf[F.v] = frag(sl(F.v)); });
    v4f aA[8] = {}, aB[6] = {};
    constexpr auto nops = [](int m) { return (m < 13 ? 1 : 0) + (m >= 8 ? 1 : 0); };
    constexpr auto sum = [=](int a, int b2) { int c = 0; for (int m = a; m < b2; ++m) c += nops(m); return c; };
    SB();
    sfor<0, 20>([&](auto N) {
      constexpr int n = N.v;
      if constexpr (n == 2 || n == 8 || n == 13) { rawbar(); SB(); }
      if constexpr (n == 17) { await1<3>(st); rawbar(); SB(); }
      await1<(n < U0 ? U0 - 1 - n + sum(0, n) : nops(n - U0) - 1 + sum(n - U0 + 1, n))>(uf[n]);
      sfor<0, 8>([&](auto T) {
        constexpr int t = T.v;
        aA[t] = mfma16(wf[t], asbf(uf[n]), aA[t]);
        wf[t] = frag(sl(8 * n + 8 + t));
        if constexpr (t == 0) {
          if constexpr (n + U0 < 20) ldu(IC<(n + U0 < 20 ? n + U0 : 0)>{});
          else if constexpr (n == 12) aloadc<0>(st, rs_ws, stoff, 0);
        }
        if constexpr (t == 4 && n >= 8) gu1(144 + 8 * (n - 8));
        SB();
      });
    });
    asm volatile("" : "+v"(aA[0]), "+v"(aA[1]), "+v"(aA[2]), "+v"(aA[3]), "+v"(aA[4]), "+v"(aA[5]), "+v"(aA[6]), "+v"(aA[7]));
    STAMP(14);
    float rs2, mr2;
    {
      const bool od = lane & 1;
      const float ma = __uint_as_float(od ? st[2] : st[0]), qa = __uint_as_float(od ? st[3] : st[1]);
      const float mb = __uint_as_float(__builtin_amdgcn_update_dpp(0u, od ? st[0] : st[2], 0xb1, 0xf, 0xf, true));
      const float qb = __uint_as_float(__builtin_amdgcn_update_dpp(0u, od ? st[1] : st[3], 0xb1, 0xf, 0xf, true));
      const float m = xsum32(xsum16(ma + mb)) * 0.125f;
      const float q = qa + qb + 80.f * ((ma - m) * (ma - m) + (mb - m) * (mb - m));
      rs2 = __builtin_amdgcn_rsqf(xsum32(xsum16(q)) * (1.f / DM) + 1e-5f); mr2 = -m * rs2;
    }
    auto geglu4 = [&](const v4f& g, const v4f& u, u32 cb) {
      const v4f ag = ldsr128f(cb), au = ldsr128f(cb + 448), bg = ldsr128f(cb + 896), bu = ldsr128f(cb + 1344);
      const v4f gv = g * rs2 + (ag * mr2 + bg), uv = u * rs2 + (au * mr2 + bu);
      const v4f z = gv * (gv * gv * (-0.044715f * 2.302208198f) + (-2.302208198f));
      v4f d = {__builtin_amdgcn_exp2f(z[0]), __builtin_amdgcn_exp2f(z[1]), __builtin_amdgcn_exp2f(z[2]), __builtin_amdgcn_exp2f(z[3])};
      d += 1.f;
      const v4f r = {__builtin_amdgcn_rcpf(d[0]), __builtin_amdgcn_rcpf(d[1]), __builtin_amdgcn_rcpf(d[2]), __builtin_amdgcn_rcpf(d[3])};
      const v4f o = gv * uv * r;
      return v2u{pkbf(o[0], o[1]), pkbf(o[2], o[3])};
    };
    const u32 abm = opq(L_AB + g4 * 32);
    const u32 ob = WS_ACT + (8 * P + wave) * 55296;
    auto epiA = [&]() {
#pragma unroll
      for (int bk = 0; bk < 2; ++bk) {
        const v2u lo = geglu4(aA[4 * bk], aA[4 * bk + 2], abm + bk * 128), hi = geglu4(aA[4 * bk + 1], aA[4 * bk + 3], abm + bk * 128 + 16);
        astorec(v4u{lo[0], lo[1], hi[0], hi[1]}, rs_ws, vlin, ob + (2 * J + bk) * 1024);
      }
    };
    auto loopB = [&](auto N0, auto N1) {
      sfor<6 * N0.v, 6 * N1.v>([&](auto I) {
        constexpr int i = I.v, sb = i / 6;
        aB[i % 6] = mfma16(wf[i % 8], asbf(uf[sb]), aB[i % 6]);
        if constexpr (i + 8 < 120) wf[i % 8] = frag(sl(160 + i + 8));
        if constexpr (sb < 5 && i % 6 == 3) gu1(240 + 8 * sb);
        if constexpr (sb >= 5 && sb < 8 && i % 6 == 3) { if constexpr (sb < 7) wd1(8 * (sb - 5) + wave); else wd1(16 + (wave & 3)); }
        if constexpr (sb >= 10 && sb < 14 && i % 3 == 1) wd1(20 + 8 * ((i - 60) / 3) + wave);
        if constexpr (sb >= 15 && sb < 19 && i % 6 == 1) wd1(84 + 8 * (sb - 15) + wave);
        if constexpr (sb >= 15 && i % 6 == 4) lda(IC<(sb >= 15 ? sb - 15 : 0)>{});
        SB();
      });
    };
    auto pinB = [&]() { asm volatile("" : "+v"(aB[0]), "+v"(aB[1]), "+v"(aB[2]), "+v"(aB[3]), "+v"(aB[4]), "+v"(aB[5])); };
    epiA();
    SB();
    loopB(IC<0>{}, IC<4>{});
    waitv<6>();
    rawbar();
    loopB(IC<4>{}, IC<7>{});
    waitv<7>();
    wflag(3);
    loopB(IC<7>{}, IC<10>{});
    waitv<4>();
    rawbar();
    STAMP(15);
    loopB(IC<10>{}, IC<15>{});
    pinB();
    if (wave == 0) poll_n(ws, WS_FLAG + (3 * 16 + P) * 512 + TH * 256, 64, tag, lane);
    rawbar();
    loopB(IC<15>{}, IC<20>{});
    pinB();
    STAMP(16);
    {
      const v2u lo = geglu4(aB[0], aB[2], abm + 256), hi = geglu4(aB[1], aB[3], abm + 272);
      astorec(v4u{lo[0], lo[1], hi[0], hi[1]}, rs_ws, vlin, ob + (32 + J) * 1024);
      const v2u hf4 = geglu4(aB[4], aB[5], L_AB + 384 + g4 * 16);
      __builtin_amdgcn_raw_buffer_store_b64(hf4, rs_ws, nt == 7 ? hh * 256 + t16 * 16 + g2 * 8 : OOB, ob + 48 * 1024 + (J - (J >> 2)) * 512, AUX_SC1);
    }
  }
  WST(14);
  sfor<5, 10>(lda);
  await4<7>(af[0], af[1], af[2], af[3]);
  asm volatile("" : "+v"(af[4]));
  rawbar();
  STAMP(17);
  {
    constexpr auto sl = [](int n, int t) { return (u32)(((n + 10) % 15) * 10240 + t * 1024); };
    v8bf wf[10];
    sfor<0, 10>([&](auto F) { wf[F.v] = frag2(sl(F.v / 5, F.v % 5)); });
    v4f acc[5] = {};
    constexpr auto nops = [](int m) { return ((m < 6 || (m >= 8 && m <= 18)) ? 1 : 0) + (m < 20 ? 1 : 0) + (m == 2 ? 1 : 0); };
    constexpr auto sum = [=](int a, int b2) { int c = 0; for (int m = a; m < b2; ++m) c += nops(m); return c; };
    SB();
    sfor<0, 27>([&](auto N) {
      constexpr int n = N.v;
      if constexpr (n == 2) { waitv<5 + sum(0, 2)>(); wflag(4); }
      if constexpr (n == 8) { if (wave == 0) poll_n(ws, WS_FLAG + (4 * 16 + P) * 512 + TH * 256, 64, tag, lane); }
      if constexpr (n >= 4 && n <= 24 && (n & 1) == 0) { if constexpr (n >= 8) waitv<sum(n - 4, n)>(); rawbar(); SB(); }
      if constexpr (n >= 5 && n < 10) await1<9 - n + sum(0, n)>(af[n]);
      if constexpr (n >= 10 && n < 16) await1<1 + sum(n - 9, n)>(af[n]);
      if constexpr (n >= 16) await1<1 + sum(n - 7, n)>(af[n]);
      sfor<0, 5>([&](auto T) {
        constexpr int t = T.v, f = (5 * n + t) % 10;
        acc[t] = mfma16(wf[f], asbf(af[n]), acc[t]);
        if constexpr (n < 25) wf[f] = frag2(sl(n + 2, t));
        if constexpr (n < 6 && t == 0) lda(IC<(n < 6 ? n + 10 : 0)>{});
        if constexpr (n >= 8 && n <= 18 && t == 0) lda(IC<(n <= 18 ? n + 8 : 0)>{});
        if constexpr (n < 20 && t == 2) { const int e = 116 + 8 * n + wave; wd1(n == 19 ? (e < 270 ? e : 269) : e); }
        SB();
      });
      if constexpr (n == 7) asm volatile("" : "+v"(acc[0]), "+v"(acc[1]), "+v"(acc[2]), "+v"(acc[3]), "+v"(acc[4]));
    });
    asm volatile("" : "+v"(acc[0]), "+v"(acc[1]), "+v"(acc[2]), "+v"(acc[3]), "+v"(acc[4]));
    STAMP(18);
    const u32 xb = 81920 + T4 * 5120 + vlin;
    if (K2 == 0) {
#pragma unroll
      for (int t = 0; t < 5; ++t) ldsw128f(xb + t * 1024, acc[t]);
    }
    bar();
    if (K2 == 1) {
      const u32 ob = 102400 + T4 * 2816;
#pragma unroll
      for (int t = 0; t < 5; ++t) {
        const v4f a = acc[t] + ldsr128f(xb + t * 1024);
        const v4f gb = ldsr128f(L_WPHI + 320 + t * 64 + g4 * 16);
        ldsw64(ob + t16 * 176 + t * 32 + g4 * 8, v2u{pkbf(x1[4 * t] + gb[0] * a[0], x1[4 * t + 1] + gb[1] * a[1]), pkbf(x1[4 * t + 2] + gb[2] * a[2], x1[4 * t + 3] + gb[3] * a[3])});
      }
#pragma unroll
      for (int i = 0; i < 3; ++i) {
        const int it = i * 64 + lane, tk = it / 10, ch = it - tk * 10;
        if (it < 160) *(GAS v4u*)((GAS char*)out + (128 * P + 64 * TH + 16 * T4 + tk) * 1280 + CT * 160 + ch * 16) = ldsr128(ob + tk * 176 + ch * 16);
      }
    }
  }
  if (b == 0 && tid == 0) __hip_atomic_store((GAS u32*)(ws + WS_EPOCH), tag, RLX_AGENT);
  WST(15);
  STAMP(19);
}
}

extern "C" void kernel_launch(void* const* d_in, const int* in_sizes, int n_in, void* d_out, int out_size, void* d_ws, size_t ws_size, hipStream_t stream) {
  (void)in_sizes; (void)n_in; (void)out_size; (void)ws_size;
  (void)hipFuncSetAttribute((const void*)fused_kernel, hipFuncAttributeMaxDynamicSharedMemorySize, L_TOTAL);
  fused_kernel<<<256, 512, L_TOTAL, stream>>>((const u16*)d_in[0], (const u16*)d_in[1], (const u16*)d_in[2], (const u16*)d_in[3], (const u16*)d_in[5],
                                              (const u16*)d_in[6], (const u16*)d_in[7], (const u16*)d_in[8], (const u16*)d_in[9], (u16*)d_out, (char*)d_ws);
}
```

```cpp
#include <hip/hip_runtime.h>
#include <stdint.h>

typedef unsigned short u16;
typedef unsigned u32;
typedef unsigned long long u64;
typedef u32 v4u __attribute__((ext_vector_type(4)));
typedef u32 v2u __attribute__((ext_vector_type(2)));
typedef float v4f __attribute__((ext_vector_type(4)));
typedef float v2f __attribute__((ext_vector_type(2)));
typedef float v16f __attribute__((ext_vector_type(16)));
typedef short v4s __attribute__((ext_vector_type(4)));
typedef __bf16 v8bf __attribute__((ext_vector_type(8)));
typedef __bf16 v2bf __attribute__((ext_vector_type(2)));
typedef _Float16 v2h __attribute__((ext_vector_type(2)));
#define LAS __attribute__((address_space(3)))
#define GAS __attribute__((address_space(1)))
#define DI __device__ __forceinline__
#define RLX_AGENT __ATOMIC_RELAXED, __HIP_MEMORY_SCOPE_AGENT
#define SB() __builtin_amdgcn_sched_barrier(0)
typedef __amdgpu_buffer_rsrc_t rsrc_t;

#define ROT_ON 1
#define NTS ""
namespace {
constexpr int SEQ = 2048, DM = 640, FF = 1728;
constexpr u32 WS_EPOCH = 0;
constexpr u32 WS_FLAG = 1024;
constexpr u32 WS_ST1 = 61440;
constexpr u32 WS_MODG = WS_ST1 + 32768;
constexpr u32 WS_AB = WS_MODG + 4 * 30720;
constexpr u32 WS_KVP = WS_AB + 4 * 3456 * 16;
constexpr u32 KVP_STRIDE = 3456;
constexpr u32 WS_STAT = WS_KVP + 256 * KVP_STRIDE;
constexpr u32 WS_O = WS_STAT + SEQ * 64;
constexpr u32 WS_U = WS_O + SEQ * DM * 2;
constexpr u32 WS_ACT = WS_U + SEQ * DM * 2;
constexpr u32 WS_GU = WS_ACT + SEQ * FF * 2;
constexpr u32 WS_WD = WS_GU + 16 * 286720;
constexpr u32 WS_WQ = WS_WD + 8 * 276480;
constexpr u32 WS_WO = WS_WQ + 16 * 153600;
constexpr u32 WS_T = WS_WO + 16 * 102400;
constexpr u32 WS_END = WS_T + SEQ * DM * 2;
constexpr u32 WS_DBG = (WS_END + 4095) & ~4095u;
static_assert(WS_KVP % 128 == 0 && WS_STAT % 128 == 0 && WS_O % 256 == 0, "align");
constexpr u32 L_RQ = 102400;
constexpr u32 L_RAW = 102400;
constexpr u32 L_MISC = 153600;
constexpr u32 L_COA = L_MISC, L_COS = L_MISC + 2560;
constexpr u32 L_KVB = L_MISC + 5120;
constexpr u32 L_ZERO = L_KVB + 3360;
constexpr u32 L_WPHI = L_ZERO + 16;
constexpr u32 L_SYNC = L_WPHI + 1600;
constexpr u32 L_TOTAL = 163840;
constexpr u32 L_G2A = L_COA, L_G2S = L_COA + 640;
constexpr u32 L_G2R = L_COA + 1280;
constexpr u32 L_AB = L_COA + 3328;
static_assert(L_SYNC + 64 <= L_TOTAL && L_AB + 1792 <= L_KVB, "lds");
constexpr u32 L_WO = 32768;
constexpr int WO_EARLY = 48, KV_THR = 8, GU_THR = 6;
#define CODE_PF 49152
#define CODE_BACK 0
constexpr u32 SPIN_CAP = 1u << 15;
constexpr int AUX_SC1 = 16;
constexpr u32 OOB = 0x7ffffff0u;

DI float bflo(u32 v) { return __uint_as_float(v << 16); }
DI float bfhi(u32 v) { return __uint_as_float(v & 0xffff0000u); }
DI u32 pkbf(float a, float b) { v2f t = {a, b}; return __builtin_bit_cast(u32, __builtin_convertvector(t, v2bf)); }
DI float rbf(float a) { return bflo(pkbf(a, a)); }
DI void bar() { asm volatile("s_waitcnt lgkmcnt(0)\n\ts_barrier" ::: "memory"); }
DI void rawbar() { asm volatile("s_barrier" ::: "memory"); }
DI void drain() { asm volatile("s_waitcnt vmcnt(0)" ::: "memory"); }
template <int N> DI void waitv() { asm volatile("s_waitcnt vmcnt(%0)" ::"i"(N) : "memory"); }
template <int I> struct IC { static constexpr int v = I; };
template <int B, int E, class F> DI void sfor(F&& f) { if constexpr (B < E) { f(IC<B>{}); sfor<B + 1, E>(f); } }
DI rsrc_t mkrs(const void* p, u32 bytes) { return __builtin_amdgcn_make_buffer_rsrc((void*)p, 0, bytes, 0x00020000); }
DI void dma16(rsrc_t rs, u32 voff, u32 soff, u32 lds) {
  asm volatile("s_mov_b32 m0, %0\n\ts_nop 0\n\tbuffer_load_dwordx4 %1, %2, %3 offen lds" ::"s"(lds), "v"(voff), "s"(rs), "s"(soff) : "memory");
}
template <int NL> DI void dmam(rsrc_t rs, u32 voff, u32 soff, u32 lds) {
  asm volatile("s_mov_b32 m0, %0\n\ts_mov_b32 exec_hi, %4\n\tbuffer_load_dwordx4 %1, %2, %3 offen" NTS " lds\n\ts_mov_b32 exec_hi, -1"
               ::"s"(lds), "v"(voff), "s"(rs), "s"(soff), "i"((1u << (NL - 32)) - 1u) : "memory");
}
DI void dma16c(rsrc_t rs, u32 voff, u32 soff, u32 lds) {
  asm volatile("s_mov_b32 m0, %0\n\ts_nop 0\n\tbuffer_load_dwordx4 %1, %2, %3 offen sc1 lds" ::"s"(lds), "v"(voff), "s"(rs), "s"(soff) : "memory");
}
template <int NL> DI void dmamc(rsrc_t rs, u32 voff, u32 soff, u32 lds) {
  asm volatile("s_mov_b32 m0, %0\n\ts_mov_b32 exec_hi, %4\n\tbuffer_load_dwordx4 %1, %2, %3 offen sc1 lds\n\ts_mov_b32 exec_hi, -1"
               ::"s"(lds), "v"(voff), "s"(rs), "s"(soff), "i"((1u << (NL - 32)) - 1u) : "memory");
}
DI u32 opq(u32 v) { asm volatile("" : "+v"(v)); return v; }
DI v4s trd(u32 addr) { return __builtin_amdgcn_ds_read_tr16_b64_v4i16((LAS v4s*)(size_t)addr); }
DI v8bf trfrag(u32 a0, u32 a1) {
  v4s a = trd(a0), b = trd(a1);
  return __builtin_bit_cast(v8bf, __builtin_shufflevector(a, b, 0, 1, 2, 3, 4, 5, 6, 7));
}
DI v16f mfma32(v8bf a, v8bf b, v16f c) { return __builtin_amdgcn_mfma_f32_32x32x16_bf16(a, b, c, 0, 0, 0); }
DI v4f mfma16(v8bf a, v8bf b, v4f c) { return __builtin_amdgcn_mfma_f32_16x16x32_bf16(a, b, c, 0, 0, 0); }
DI v8bf asbf(v4u v) { return __builtin_bit_cast(v8bf, v); }
DI v4u ldsr128(u32 a) { return *(LAS v4u*)(size_t)a; }
DI v4f ldsr128f(u32 a) { return *(LAS v4f*)(size_t)a; }
DI v2u ldsr64(u32 a) { return *(LAS v2u*)(size_t)a; }
DI void ldsw128(u32 a, v4u v) { *(LAS v4u*)(size_t)a = v; }
DI void ldsw128f(u32 a, v4f v) { *(LAS v4f*)(size_t)a = v; }
DI void ldsw64(u32 a, v2u v) { *(LAS v2u*)(size_t)a = v; }
DI float ldsrf(u32 a) { return *(LAS float*)(size_t)a; }
DI void ldswf(u32 a, float v) { *(LAS float*)(size_t)a = v; }
DI float mixlo(u32 t, float a, float s) { float r; asm("v_fma_mix_f32 %0, %1, %2, %3 op_sel_hi:[1,0,0]" : "=v"(r) : "v"(t), "v"(a), "v"(s)); return r; }
DI float mixhi(u32 t, float a, float s) { float r; asm("v_fma_mix_f32 %0, %1, %2, %3 op_sel:[1,0,0] op_sel_hi:[1,0,0]" : "=v"(r) : "v"(t), "v"(a), "v"(s)); return r; }
DI float xsum16(float v) {
  auto r = __builtin_amdgcn_permlane16_swap(__float_as_uint(v), __float_as_uint(v), false, false);
  return __uint_as_float(r[0]) + __uint_as_float(r[1]);
}
DI float xsum32(float v) {
  auto r = __builtin_amdgcn_permlane32_swap(__float_as_uint(v), __float_as_uint(v), false, false);
  return __uint_as_float(r[0]) + __uint_as_float(r[1]);
}
template <int CTRL> DI float dppadd(float v) {
  return v + __uint_as_float(__builtin_amdgcn_update_dpp(0u, __float_as_uint(v), CTRL, 0xf, 0xf, true));
}
constexpr int DPP_ROR4 = 0x124, DPP_ROR8 = 0x128;
DI float red8(const float (&a)[8], int lane) {
  float b4[4], c[2];
#pragma unroll
  for (int e = 0; e < 4; ++e) { auto r = __builtin_amdgcn_permlane32_swap(__float_as_uint(a[e]), __float_as_uint(a[4 + e]), false, false); b4[e] = __uint_as_float(r[0]) + __uint_as_float(r[1]); }
#pragma unroll
  for (int e = 0; e < 2; ++e) { auto r = __builtin_amdgcn_permlane16_swap(__float_as_uint(b4[e]), __float_as_uint(b4[2 + e]), false, false); c[e] = __uint_as_float(r[0]) + __uint_as_float(r[1]); }
  const float x = dppadd<DPP_ROR8>(c[0]), y = dppadd<DPP_ROR8>(c[1]);
  return (lane & 8) ? y : x;
}
DI v4u ldb128(rsrc_t rs, u32 vo, u32 so) { return __builtin_amdgcn_raw_buffer_load_b128(rs, vo, so, 0); }
DI v4u ldb128c(rsrc_t rs, u32 vo, u32 so) { return __builtin_amdgcn_raw_buffer_load_b128(rs, vo, so, AUX_SC1); }
template <int OFF> DI void aload(v4u& r, rsrc_t rs, u32 vo, u32 so) {
  asm volatile("buffer_load_dwordx4 %0, %1, %2, %3 offen offset:%4" NTS : "=v"(r) : "v"(vo), "s"(rs), "s"(so), "i"(OFF) : "memory");
}
template <int OFF> DI void aloadc(v4u& r, rsrc_t rs, u32 vo, u32 so) {
  asm volatile("buffer_load_dwordx4 %0, %1, %2, %3 offen offset:%4 sc1" : "=v"(r) : "v"(vo), "s"(rs), "s"(so), "i"(OFF) : "memory");
}
template <int N> DI void await1(v4u& a) { asm volatile("s_waitcnt vmcnt(%1)" : "+v"(a) : "i"(N) : "memory"); }
template <int N> DI void await2(v4u& a, v4u& b) { asm volatile("s_waitcnt vmcnt(%2)" : "+v"(a), "+v"(b) : "i"(N) : "memory"); }
template <int N> DI void await4(v4u& a, v4u& b, v4u& c, v4u& d) { asm volatile("s_waitcnt vmcnt(%4)" : "+v"(a), "+v"(b), "+v"(c), "+v"(d) : "i"(N) : "memory"); }
DI void astorec(v4u v, rsrc_t rs, u32 vo, u32 so) { u32 o = vo + so; asm volatile("" : "+v"(o)); __builtin_amdgcn_raw_buffer_store_b128(v, rs, o, 0, AUX_SC1); }
DI int sopq(int v) { asm volatile("" : "+s"(v)); return v; }
template <int N> DI void await3(v4u& a, v4u& b, v4u& c) { asm volatile("s_waitcnt vmcnt(%3)" : "+v"(a), "+v"(b), "+v"(c) : "i"(N) : "memory"); }
DI void stb128c(rsrc_t rs, u32 vo, v4u v) { __builtin_amdgcn_raw_buffer_store_b128(v, rs, vo, 0, AUX_SC1); }
DI void stb64c(rsrc_t rs, u32 vo, v2u v) { __builtin_amdgcn_raw_buffer_store_b64(v, rs, vo, 0, AUX_SC1); }

#define WST(i) do {} while (0)
#define STAMP(i) do {} while (0)

DI void setflag(char* ws, u32 off, u32 tag) { (void)__hip_atomic_exchange((GAS u32*)(ws + off), tag, RLX_AGENT); }
DI void bflag(rsrc_t rs, u32 tag, u32 off) { asm volatile("s_mov_b64 exec, 1\n\tbuffer_atomic_swap %0, off, %2, %1\n\ts_mov_b64 exec, -1" ::"v"(tag), "s"(off), "s"(rs) : "memory"); }
DI void publish(char* ws, u32 flagoff, u32 tag) {
  drain();
  bar();
  if (threadIdx.x == 0) setflag(ws, flagoff, tag);
}
DI void poll_n(char* ws, u32 off, int n, u32 tag, int lane) {
  GAS u32* f = (GAS u32*)(ws + off) + lane;
#pragma unroll 1
  for (u32 spin = 0; spin < SPIN_CAP; ++spin) {
    u32 v = tag;
    if (lane < n) { v = 0; asm volatile("global_atomic_or %0, %1, %0, off sc0 sc1\n\ts_waitcnt vmcnt(0)" : "+v"(v) : "v"(f) : "memory"); }
    if (__all(v == tag)) break;
    __builtin_amdgcn_s_sleep(1);
  }
}
DI void lds_post(u32 a, u32 tag) { asm volatile("" ::: "memory"); *(volatile LAS u32*)(size_t)a = tag; }
DI void lds_wait(u32 a, u32 tag) {
#pragma unroll 1
  for (u32 spin = 0; spin < (SPIN_CAP << 4); ++spin) {
    if (__all(*(volatile LAS u32*)(size_t)a >= tag)) break;
    __builtin_amdgcn_s_sleep(2);
  }
  asm volatile("" ::: "memory");
}
DI void gather2(rsrc_t rs, u32 o1, u32 o2, u32 tag, v4u& g1, v4u& g2) {
#pragma unroll 1
  for (u32 spin = 0; spin < SPIN_CAP; ++spin) {
    asm volatile("" ::: "memory");
    g1 = ldb128c(rs, o1, 0);
    g2 = ldb128c(rs, o2, 0);
    if (__all(g1[1] == tag && g1[3] == tag && g2[1] == tag && g2[3] == tag)) break;
    __builtin_amdgcn_s_sleep(1);
  }
}
DI void gather4(rsrc_t rs, u32 o, u32 st, u32 tag, v4u& g0, v4u& g1, v4u& g2, v4u& g3) {
#pragma unroll 1
  for (u32 spin = 0; spin < SPIN_CAP; ++spin) {
    asm volatile("" ::: "memory");
    g0 = ldb128c(rs, o, 0); g1 = ldb128c(rs, o, st); g2 = ldb128c(rs, o, 2 * st); g3 = ldb128c(rs, o, 3 * st);
    if (__all(g0[1] == tag && g0[3] == tag && g1[1] == tag && g1[3] == tag && g2[1] == tag && g2[3] == tag && g3[1] == tag && g3[3] == tag)) break;
    __builtin_amdgcn_s_sleep(1);
  }
}
DI v2f sum4(const v4u& a, const v4u& b, const v4u& c, const v4u& d, float one) {
  const float x = (__uint_as_float(a[0]) + __uint_as_float(b[0])) + (__uint_as_float(c[0]) + __uint_as_float(d[0]));
  const float y = (__uint_as_float(a[2]) + __uint_as_float(b[2])) + (__uint_as_float(c[2]) + __uint_as_float(d[2]));
  return v2f{rbf(one + rbf(x)), rbf(one + rbf(y))};
}
DI bool tags4(const v4u& a, const v4u& b, const v4u& c, const v4u& d, u32 t) {
  return __all(a[1] == t && a[3] == t && b[1] == t && b[3] == t && c[1] == t && c[3] == t && d[1] == t && d[3] == t);
}
DI v4f unp4(v4u g, float one) { return v4f{rbf(one + bflo(g[0])), rbf(one + bfhi(g[0])), rbf(one + bflo(g[2])), rbf(one + bfhi(g[2]))}; }

DI void softmax20(const float (&p)[20], u32 (&o)[10]) {
  float mx = p[0];
#pragma unroll
  for (int i = 1; i < 20; ++i) mx = fmaxf(mx, p[i]);
  const float L2E = 1.4426950408889634f, a = -mx * L2E;
  float e[20], s0 = 0.f, s1 = 0.f;
#pragma unroll
  for (int i = 0; i < 20; ++i) { e[i] = __builtin_amdgcn_exp2f(__builtin_fmaf(p[i], L2E, a)); if (i & 1) s1 += e[i]; else s0 += e[i]; }
  const float r = __builtin_amdgcn_rcpf(s0 + s1);
#pragma unroll
  for (int i = 0; i < 10; ++i) o[i] = pkbf(e[2 * i] * r, e[2 * i + 1] * r);
}
__global__ __launch_bounds__(512, 2) void fused_kernel(const u16* __restrict__ x, const u16* __restrict__ wqkv, const u16* __restrict__ wphi,
                                                       const u16* __restrict__ wo, const u16* __restrict__ wg, const u16* __restrict__ wu,
                                                       const u16* __restrict__ wd, const u16* __restrict__ cvec, const u16* __restrict__ wmod,
                                                       u16* __restrict__ out, char* ws) {
  const int tid = threadIdx.x, lane = tid & 63;
  const int wave = __builtin_amdgcn_readfirstlane(tid >> 6);
  const int b = blockIdx.x, xcd = b & 7, idx = b >> 3;
  __builtin_assume(wave >= 0 && wave < 8);
  __builtin_assume(b >= 0 && b < 256);
  {
    const rsrc_t rs_cd = mkrs((const void*)(__builtin_amdgcn_s_getpc() & ~(u64)127), CODE_PF);
    asm volatile("s_mov_b32 m0, 0\n\ts_mov_b64 exec, 3\n\tbuffer_load_dword %0, %1, %2 offen lds\n\ts_mov_b64 exec, -1" ::"v"(lane * 32768), "s"(rs_cd), "s"((wave * 32 + idx) * 128) : "memory");
  }
  SB();
  STAMP(0);
  const int T4 = wave & 3, K2 = wave >> 2;
  const int mc = b >> 2, mk = b & 3;
  const u32 coff2 = (160 * mk + 40 * T4 + 5 * (lane >> 3)) * 2;
  const int P = 4 * (xcd >> 1) + (idx >> 3);
  const int J = 8 * (xcd & 1) + (idx & 7);
  const int TH = J >> 3, CT = J & 7;
  const rsrc_t rs_ws = mkrs(ws, WS_END);
  const rsrc_t rs_x = mkrs(x, SEQ * DM * 2);
  const u32 vlin = lane * 16;
  const int hh = lane >> 5, i32 = lane & 31, g4 = lane >> 4, t16 = lane & 15;
  const int g2 = g4 & 1, q4 = t16 >> 2, p4 = lane & 3;
  v4u wm[5], cq, tagq;
  {
    const rsrc_t rs_a = mkrs(K2 ? x : wmod, K2 ? SEQ * DM * 2 : DM * 6 * DM * 2), rs_c = mkrs(cvec, DM * 2);
    const u32 vo = K2 ? (128 * P + 8 * J + 2 * T4 + hh) * 1280 + i32 * 16 : coff2 * 3840 + mc * 128 + (lane & 7) * 16;
    aload<0>(cq, rs_c, coff2 & ~3u, 0);
    sfor<0, 5>([&](auto I) { aload<0>(wm[I.v], rs_a, vo, K2 ? (I.v < 3 ? I.v * 512 : 0x10000000) : I.v * 7680); });
    aloadc<0>(tagq, rs_ws, WS_EPOCH, 0);
  }
  SB();
  ldsw128(L_SYNC + (lane & 3) * 16, v4u{0u, 0u, 0u, 0u});
  ldsw128(L_ZERO, v4u{0u, 0u, 0u, 0u});
  const int rot32 = (idx & 7) * 4 + (idx >> 3);
  int R20 = (rot32 * 20) >> 5;
  const int R4A = rot32 >> 1, R4B = (rot32 * 11) >> 5;
  auto wrap = [](int v, int m) { return v >= m ? v - m : v; };
  const int t0 = 7 * J - (J >> 2), nt = (J & 3) == 3 ? 6 : 7;
  const int rq27 = 2 * P + TH;
  const u32 wqs = WS_WQ + J * 153600;
  auto tile = [&](u32 src, u32 rstride, u32 dst, u32 so) {
    astorec(__builtin_bit_cast(v4u, trfrag(src + q4 * rstride, src + (4 + q4) * rstride)), rs_ws, dst, so);
  };
  const int l10 = lane / 10, c10 = lane - l10 * 10;
  const u32 v10 = l10 * 1280 + c10 * 16;
  constexpr u32 L_R2 = L_RAW + 11520;
  constexpr u32 L_RD = L_R2 + 17920;
  auto rawloads = [&]() {
    const rsrc_t rs_d = mkrs(wd, FF * DM * 2);
    const int row = lane / 14; int ch = lane - row * 14; if (ch >= 2 * nt) ch -= 2;
    const u32 vg = row * 3456 + ch * 16;
#pragma unroll 1
    for (int m = 0; m < 2; ++m) {
      const rsrc_t rs_g = mkrs(m ? wu : wg, DM * FF * 2);
#pragma unroll 1
      for (int i = 0; i < 10; ++i) dmam<56>(rs_g, vg, (40 * P + 4 * i) * 3456 + t0 * 32, L_R2 + m * 8960 + i * 896);
    }
    if (rq27 < 27) {
#pragma unroll 1
      for (int z = 0; z < 4; ++z) {
        const int o = 8 * rq27 + 2 * z;
        int jj, oo;
        if (o < 128) { jj = o >> 3; oo = o & 7; } else if (o < 192) { jj = (o - 128) >> 2; oo = 8 + (o & 3); } else { const int hq = (o - 192) >> 1; jj = hq + hq / 3; oo = 12; }
        const int wdrow = (7 * jj - (jj >> 2)) * 16 + oo * 8;
#pragma unroll 1
        for (int i = 0; i < 4; ++i) dmam<40>(rs_d, v10, (wdrow + 4 * i) * 1280 + CT * 160, L_RD + z * 2560 + i * 640);
      }
    }
  };
  constexpr u32 L_WR = 81920, L_OR = 92160;
  u32 ta[2] = {}, td[2][2] = {};
  if (K2) {
    {
      const rsrc_t rs_w = mkrs(wqkv, DM * 3 * DM * 2), rs_2 = mkrs(T4 == 1 ? wphi : wqkv, T4 == 1 ? 32 * 400 * 2 : DM * 3 * DM * 2);
      const int r = lane / 15, c = lane - r * 15, pt = c / 5;
      const u32 vo = r * 3840 + pt * 1280 + (c - pt * 5) * 16, so = (40 * P + 8 * T4) * 3840 + J * 80;
      dmam<60>(rs_w, vo, so, L_WR + T4 * 1920); dmam<60>(rs_w, vo, so + 4 * 3840, L_WR + T4 * 1920 + 960);
      const u32 v2 = T4 == 1 ? vlin : vo, s2 = T4 == 1 ? J * 1600 : (40 * P + 32) * 3840 + J * 80;
      dmam<60>(rs_2, v2, s2, T4 == 1 ? L_WPHI : L_WR + 7680); dmam<60>(rs_2, v2, s2 + (T4 == 1 ? 640 : 4 * 3840), T4 == 1 ? L_WPHI + 640 : L_WR + 8640);
    }
  {
    const u32 cm = (p4 >> 1) * 40 + (p4 & 1) * 16;
    ta[0] = opq(L_WR + q4 * 240 + 80 + hh * 80 + cm + g2 * 8);
    ta[1] = opq(L_WR + q4 * 240 + (g4 == 0 ? 80 + (p4 & 1) * 80 + (p4 >> 1) * 40 + 32 : g4 == 3 ? (p4 & 1) * 40 + 32 : cm + (g4 - 1) * 8));
    const u32 sA = g4 == 0 ? 5120 : 2560, sB = g4 == 3 ? 128 : 256, sC = (g4 == 0 ? 4096 : (g4 == 3 && t16 >= 8) ? OOB : 102400 + (g4 - 1) * 1024) + t16 * 16;
#pragma unroll
    for (int z = 0; z < 2; ++z) {
      const int oct = 5 * P + (z ? 4 : T4);
      td[z][0] = opq(wqs + (oct >> 2) * 5120 + (oct & 3) * 256 + g4 * 1024 + t16 * 16);
      td[z][1] = opq(wqs + (oct >> 2) * sA + (oct & 3) * sB + sC);
    }
  }
  }
  u32 tag;
  bar();
  await1<6>(cq);
  float sv[5];
  {
    const u32 sh = (coff2 & 2) * 8;
    const u32 ce[3] = {__builtin_amdgcn_alignbit(cq[1], cq[0], sh), __builtin_amdgcn_alignbit(cq[2], cq[1], sh), cq[2] >> sh};
#pragma unroll
    for (int i = 0; i < 5; ++i) {
      const float cv = (i & 1) ? bfhi(ce[i >> 1]) : bflo(ce[i >> 1]);
      sv[i] = rbf(cv * __builtin_amdgcn_rcpf(1.f + __builtin_amdgcn_exp2f(-1.4426950408889634f * cv)));
      asm volatile("" : "+v"(sv[i]));
    }
  }
  await1<0>(tagq);
  asm volatile("" : "+v"(wm[0]), "+v"(wm[1]), "+v"(wm[2]), "+v"(wm[3]), "+v"(wm[4]));
  tag = tagq[0] + 1u;
  WST(0);
  if (!K2) {
    float acc[8] = {0.f, 0.f, 0.f, 0.f, 0.f, 0.f, 0.f, 0.f};
#pragma unroll
    for (int i = 0; i < 5; ++i)
#pragma unroll
      for (int e = 0; e < 4; ++e) { acc[2 * e] += sv[i] * bflo(wm[i][e]); acc[2 * e + 1] += sv[i] * bfhi(wm[i][e]); }
    ldswf(L_KVB + T4 * 256 + ((lane & 7) * 8 + 4 * hh + 2 * g2 + ((lane >> 3) & 1)) * 4, red8(acc, lane));
    if (wave) lds_post(L_SYNC + 12 + wave * 4, 1u);
    else {
      lds_wait(L_SYNC + 16 + (lane % 3) * 4, 1u);
      const u32 a = L_KVB + lane * 4;
      const float v = (ldsrf(a) + ldsrf(a + 256)) + (ldsrf(a + 512) + ldsrf(a + 768));
      stb64c(rs_ws, b < 240 ? WS_MODG + (mk * 3840 + mc * 64 + lane) * 8 : OOB, v2u{__float_as_uint(v), tag});
      drain();
      const int vec = mc >= 10;
      if (mc < 20 && lane < 32 && ((40 * t16 + 39 * g2) >> 6) == mc - 10 * vec) setflag(ws, WS_FLAG + (6 * 16 + t16) * 512 + 256 + (vec * 8 + g2 * 4 + mk) * 4, tag);
    }
    WST(1);
    {
      const int r5 = lane / 5;
      const u32 vs = r5 * 1280 + (lane - r5 * 5) * 16, so = (128 * P + 32 * T4) * 1280 + J * 80, lx = L_RAW + T4 * 2880;
      dmam<60>(rs_x, vs, so, lx); dmam<60>(rs_x, vs, so + 12 * 1280, lx + 960); dmam<40>(rs_x, vs, so + 24 * 1280, lx + 1920);
    }
    if (wave == 1) rawloads();
    if (wave == 2) {
      const int e = lane < 40 ? lane : 39, vec = e >= 20;
      v4u g0, g1, g2v, g3;
      poll_n(ws, WS_FLAG + (6 * 16 + J) * 512 + 256, 16, tag, lane);
      gather4(rs_ws, WS_MODG + (vec * 640 + 40 * J) * 8 + (e - 20 * vec) * 16, 30720, tag, g0, g1, g2v, g3);
      *(LAS v2f*)(size_t)(L_COA + e * 8) = sum4(g0, g1, g2v, g3, vec ? 1.f : 0.f);
      lds_post(L_SYNC, 1u);
    } else if (wave == 3) {
      v4u g0, g1;
      poll_n(ws, WS_FLAG + (5 * 16 + P) * 512 + 256, 64, tag, lane);
      gather2(rs_ws, WS_ST1 + P * 2048 + vlin, WS_ST1 + P * 2048 + 1024 + vlin, tag, g0, g1);
      ldsw64(L_COS + lane * 8, v2u{g0[0], g0[2]}); ldsw64(L_COS + 512 + lane * 8, v2u{g1[0], g1[2]});
      lds_post(L_SYNC + 4, 1u);
    }
    drain();
    v4u xs[3];
    xs[0] = ldsr128(L_RAW + T4 * 2880 + t16 * 80 + g4 * 16); xs[1] = ldsr128(L_RAW + T4 * 2880 + 1280 + t16 * 80 + g4 * 16); xs[2] = ldsr128(L_RAW + T4 * 2880 + i32 * 80 + 64);
    WST(4);
    lds_wait(L_SYNC + (lane & 1) * 4, 1u);
    const u32 cw = L_COA;
    const v2u s0 = ldsr64(L_COS + (32 * T4 + t16) * 8), s1 = ldsr64(L_COS + (32 * T4 + 16 + t16) * 8), s2 = g2 ? s1 : s0;
    auto hch = [&](const v4u& xv, u32 cb, u32 mu, u32 ru) {
      const float rs = __uint_as_float(ru), mr = -__uint_as_float(mu) * rs;
      v4u o;
#pragma unroll
      for (int z = 0; z < 2; ++z) {
        const v4f S = ldsr128f(cb + z * 16), A = ldsr128f(cb + 160 + z * 16);
        o[2 * z] = pkbf(__builtin_fmaf(__builtin_fmaf(bflo(xv[2 * z]), rs, mr), A[0], S[0]), __builtin_fmaf(__builtin_fmaf(bfhi(xv[2 * z]), rs, mr), A[1], S[1]));
        o[2 * z + 1] = pkbf(__builtin_fmaf(__builtin_fmaf(bflo(xv[2 * z + 1]), rs, mr), A[2], S[2]), __builtin_fmaf(__builtin_fmaf(bfhi(xv[2 * z + 1]), rs, mr), A[3], S[3]));
      }
      return o;
    };
    const u32 tb = WS_T + ((8 * P + 2 * T4) * 80 + 5 * J) * 256;
    stb128c(rs_ws, tb + vlin, hch(xs[0], cw + g4 * 32, s0[0], s0[1]));
    stb128c(rs_ws, tb + 20480 + vlin, hch(xs[1], cw + g4 * 32, s1[0], s1[1]));
    stb128c(rs_ws, lane < 32 ? tb + g2 * 20480 + 1024 + t16 * 16 : OOB, hch(xs[2], cw + 128, s2[0], s2[1]));
    drain();
    if (lane == 0) setflag(ws, WS_FLAG + (6 * 16 + P) * 512 + (T4 * 16 + J) * 4, tag);
    WST(3);
    STAMP(1);
  } else {
    {
      const u32 ro = T4 * 1920;
      const v8bf f0 = trfrag(ta[0] + ro, ta[0] + ro + 960), f1 = trfrag(ta[1] + ro, ta[1] + ro + 960);
      const u32 tz = (T4 >> 1) ? ta[T4 & 1] : ta[0];
      const v8bf f2 = trfrag(tz + 7680, tz + 8640);
      stb128c(rs_ws, td[0][0], __builtin_bit_cast(v4u, f0)); stb128c(rs_ws, td[0][1], __builtin_bit_cast(v4u, f1));
      stb128c(rs_ws, (T4 >> 1) ? ((T4 & 1) ? td[1][1] : td[1][0]) : OOB, __builtin_bit_cast(v4u, f2));
    }
    {
      float tn[24];
#pragma unroll
      for (int z = 0; z < 3; ++z)
#pragma unroll
        for (int e = 0; e < 4; ++e) { tn[8 * z + 2 * e] = bflo(wm[z][e]); tn[8 * z + 2 * e + 1] = bfhi(wm[z][e]); }
      auto hsum = [](float a) { return xsum16(dppadd<DPP_ROR8>(dppadd<DPP_ROR4>(dppadd<0x4e>(dppadd<0xb1>(a))))); };
      float sm = 0.f, sm2 = 0.f;
#pragma unroll
      for (int e = 0; e < 24; ++e) { if (e < 16) sm += tn[e]; else sm2 += tn[e]; }
      const float m = hsum(i32 < 16 ? sm + sm2 : sm) * (1.f / DM);
      float q = 0.f, q2 = 0.f;
#pragma unroll
      for (int e = 0; e < 24; ++e) { const float d = tn[e] - m; if (e < 16) q += d * d; else q2 += d * d; }
      const float rs = __builtin_amdgcn_rsqf(hsum(i32 < 16 ? q + q2 : q) * (1.f / DM) + 1e-5f);
      stb64c(rs_ws, i32 < 2 ? WS_ST1 + (128 * P + 8 * J + 2 * T4 + hh) * 16 + i32 * 8 : OOB, v2u{__float_as_uint(i32 ? rs : m), tag});
    }
    drain();
    if (lane < 2) setflag(ws, WS_FLAG + (lane ? (5 * 16 + J) * 512 + (P * 4 + T4) * 4 : (5 * 16 + P) * 512 + 256 + (J * 4 + T4) * 4), tag);
    WST(1);
    if (wave == 4) { poll_n(ws, WS_FLAG + (5 * 16 + J) * 512, 64, tag, lane); lds_post(L_SYNC + 8, 1u); }
    else {
      const rsrc_t rs_wo = mkrs(wo, DM * DM * 2);
      const int o0 = 2 * T4 - 2;
      const u32 so = (40 * P + 8 * o0) * 1280 + CT * 160, lr = L_OR + o0 * 1280;
      dmam<40>(rs_wo, v10, so, lr); dmam<40>(rs_wo, v10, so + 4 * 1280, lr + 640);
      if (T4 < 3) { dmam<40>(rs_wo, v10, so + 8 * 1280, lr + 1280); dmam<40>(rs_wo, v10, so + 12 * 1280, lr + 1920); }
      drain();
#pragma unroll 1
      for (int z = 0; z < 3; ++z) {
        const int ul = 4 * z + g4, hi = ul >= 5, tl = ul - 5 * hi, oct = 5 * P + o0 + hi;
        const bool ok = ul < (T4 < 3 ? 10 : 5);
        tile(lr + (ok ? hi * 1280 + tl * 32 : 0) + p4 * 8, 160, ok ? (oct >> 2) * 5120 + tl * 1024 + (oct & 3) * 256 + t16 * 16 : OOB, WS_WO + J * 102400);
      }
      lds_post(L_SYNC + 32 + wave * 4, 1u);
      lds_wait(L_SYNC + 8, 1u);
    }
    WST(3);
#pragma unroll 1
    for (int e = T4; e < 100; e += 4) {
      if (e >= 88 && e < 92) lds_wait(L_SYNC + 52 + (lane % 3) * 4, 1u);
      dma16c(rs_ws, vlin, wqs + wrap(e + 5 * R20, 100) * 1024, e * 1024);
    }
    WST(4);
  }
  WST(5);
  STAMP(2);
  u32 ts0[4], ts1[4], tdd[4];
#pragma unroll
  for (int z = 0; z < 4; ++z) {
    const int k = wave + 8 * z;
    u32 src, rst, dst;
    if (k < 18) {
      const bool blk = k < 15;
      const int o5b = k / 3, bk = k - o5b * 3, o5 = blk ? o5b : 2 * (k - 15) + hh, oct = 5 * P + o5;
      src = L_R2 + (o5 < 5 ? o5 : 0) * 1792 + (blk ? hh * 8960 + bk * 64 + p4 * 16 + g2 * 8 : g2 * 8960 + 192 + p4 * 8); rst = 224;
      dst = WS_GU + J * 286720 + (oct & 3) * 256 + t16 * 16 + (blk ? (bk < 2 ? (oct >> 2) * 8192 + bk * 4096 : 163840 + (oct >> 2) * 6144) + g4 * 1024 : 163840 + (oct >> 2) * 6144 + 4096 + g2 * 1024);
      dst = o5 < 5 ? dst : OOB;
    } else {
      const int kk = k < 28 ? k - 18 : 0, og = kk >= 5, tl = kk - 5 * og;
      src = L_RD + (4 * og + g4) * 1280 + tl * 32 + p4 * 8; rst = 160;
      dst = (k < 28 && rq27 < 27) ? WS_WD + CT * 276480 + (2 * rq27 + og) * 5120 + tl * 1024 + vlin : OOB;
    }
    ts0[z] = opq(src + q4 * rst); ts1[z] = opq(src + (4 + q4) * rst); tdd[z] = opq(dst);
  }
  if (wave == 0) poll_n(ws, WS_FLAG + (6 * 16 + P) * 512, 64, tag, lane);
  drain();
  bar();
  WST(6);
  STAMP(3);
  v4u hf[20];
  u32 lb[3];
  lb[0] = opq(vlin); lb[1] = opq(vlin + 51200); lb[2] = opq(vlin + 102400);
  auto frag = [&](u32 off) { return asbf(ldsr128(lb[off / 51200] + off % 51200)); };
  v4f aC[5] = {};
  v4u wpf[5][2];
  {
    constexpr int H0 = 8;
    const u32 hvo = WS_T + (8 * P + wave) * 20480 + vlin;
    auto ldh = [&](auto N) { aloadc<0>(hf[N.v], rs_ws, hvo, wrap(N.v + R20, 20) * 1024); };
    sfor<0, H0>(ldh);
  {
    const u32 mk0 = (q4 >> 1) == hh ? 0xffffffffu : 0u, mk1 = g2 ? 0u : mk0, sgn = (q4 & 1) ? 0x80008000u : 0u;
    const u32 r0 = L_WPHI + hh * 800 + q4 * 40;
#pragma unroll
    for (int T = 0; T < 5; ++T) {
      const u32 c = ((p4 & 1) ? (T + 1) % 5 : T) * 8;
      wpf[T][0] = __builtin_bit_cast(v4u, trfrag(r0 + g2 * 320 + c, r0 + g2 * 320 + 160 + c));
      const v2u l = __builtin_bit_cast(v2u, trd(r0 + 640 + c));
#pragma unroll
      for (int e = 0; e < 4; ++e) wpf[T][0][e] = (wpf[T][0][e] ^ sgn) & mk0;
      wpf[T][1] = v4u{(l[0] ^ sgn) & mk1, (l[1] ^ sgn) & mk1, 0u, 0u};
      asm volatile("" : "+v"(wpf[T][0]), "+v"(wpf[T][1]));
    }
  }
    v8bf wf[10], tfr;
    sfor<0, 10>([&](auto F) { wf[F.v] = frag(F.v * 1024); });
    SB();
    sfor<0, 20>([&](auto N) {
      constexpr int n = N.v;
      constexpr auto nst = [](int a, int b2) { int c = 0; for (int m = a < 0 ? 0 : a; m < b2; ++m) c += (m >= 2 && m < 10 && (m & 1) == 1) ? 1 : 0; return c; };
      if constexpr (n == 12) { rawbar(); SB(); }
      await1<7 + nst(n - 8, n)>(hf[n]);
      sfor<0, 5>([&](auto T) {
        constexpr int t = T.v, f = (5 * n + t) % 10;
        aC[t] = mfma16(wf[f], asbf(hf[n]), aC[t]);
        if constexpr (n < 18) wf[f] = frag((5 * n + 10 + t) * 1024);
        if constexpr (t == 0 && n < 12) ldh(IC<(n < 12 ? n + H0 : 0)>{});
        if constexpr (t == 2 && n >= 2 && n < 10) {
          constexpr int z = (n - 2) >> 1;
          if constexpr ((n & 1) == 0) tfr = trfrag(ts0[z], ts1[z]); else stb128c(rs_ws, tdd[z], __builtin_bit_cast(v4u, tfr));
        }
        if constexpr (t == 0 && n >= 12 && n < 19) {
          const int e = n < 18 ? 8 * (n - 12) + wave : 48 + (wave & 1);
          dma16c(rs_ws, vlin, wqs + 102400 + wrap(e + 5 * (R20 >> 1), 50) * 1024, L_RQ + e * 1024);
        }
        SB();
      });
    });
    asm volatile("" : "+v"(aC[0]), "+v"(aC[1]), "+v"(aC[2]), "+v"(aC[3]), "+v"(aC[4]));
  }
  WST(7);
  STAMP(4);
  auto phi = [&](const v4f& m0, const v4f& m1, const v4f& l, u32 (&o)[10]) {
    const v4u b0 = {pkbf(m0[0], m0[1]), pkbf(m0[2], m0[3]), pkbf(m1[0], m1[1]), pkbf(m1[2], m1[3])};
    const v4u b1 = {pkbf(l[0], l[1]), pkbf(l[2], l[3]), 0u, 0u};
    float pv[20];
#pragma unroll
    for (int T = 0; T < 5; ++T) {
      v4f d = {};
      d = mfma16(asbf(wpf[T][0]), asbf(b0), d); d = mfma16(asbf(wpf[T][1]), asbf(b1), d);
#pragma unroll
      for (int e = 0; e < 4; ++e) pv[4 * T + e] = d[e];
    }
    softmax20(pv, o);
  };
  {
    u32 pk[10];
    phi(aC[0], aC[1], aC[4], pk);
    const int tok = 16 * wave + t16;
    const u32 fx = (((tok >> 1) & 1) << 2) | ((tok & 1) << 1) | ((tok >> 2) & 1), rb = hh * 16384 + tok * 128;
    auto cad = [&](int cc) { return rb + ((cc ^ fx) << 4); };
    bar();
    ldsw128(cad(g2), v4u{pkbf(aC[2][0], aC[2][1]), pkbf(aC[2][2], aC[2][3]), pkbf(aC[3][0], aC[3][1]), pkbf(aC[3][2], aC[3][3])});
    if (g2) ldsw128(cad(2), v4u{pkbf(aC[4][0], aC[4][1]), pkbf(aC[4][2], aC[4][3]), 0x00003f80u, 0u});
    ldsw128(cad(g2 ? 7 : 4), v4u{pk[0], pk[1], pk[2], pk[3]});
    ldsw128(cad(g2 ? 3 : 5), v4u{pk[4], pk[5], pk[6], pk[7]});
    ldsw64(cad(6) + g2 * 8, v2u{pk[8], pk[9]});
  }
  drain();
  bar();
  STAMP(5);
  if (wave < 4) {
    const int hd = wave >> 1, tl = wave & 1;
    u32 av[2], ap[2];
#pragma unroll
    for (int t = 0; t < 2; ++t) {
      const int tok = 8 * hh + 4 * t + q4;
      const u32 fx = (((tok >> 1) & 1) << 2) | ((tok & 1) << 1) | ((tok >> 2) & 1), cl = 2 * g2 + (p4 >> 1);
      av[t] = hd * 16384 + tok * 128 + ((cl ^ fx) << 4) + 8 * (p4 & 1);
      ap[t] = hd * 16384 + tok * 128 + ((((tl ? 0 : 4) + cl) ^ fx) << 4) + 8 * (p4 & 1);
    }
    v8bf fv[8], fp[8];
#pragma unroll
    for (int s = 0; s < 8; ++s) { fp[s] = trfrag(ap[0] + s * 2048, ap[1] + s * 2048); fv[s] = trfrag(av[0] + s * 2048, av[1] + s * 2048); }
    v16f d = {};
#pragma unroll
    for (int s = 0; s < 8; ++s) d = mfma32(fp[s], fv[s], d);
    const u32 base = i32 < 21 ? WS_KVP + (J * 16 + P) * KVP_STRIDE + hd * 1680 + i32 * 8 + hh * 168 : OOB;
    if (tl == 0) {
#pragma unroll
      for (int rq = 0; rq < 3; ++rq) stb64c(rs_ws, base + rq * 336, v2u{pkbf(d[4 * rq], d[4 * rq + 1]), pkbf(d[4 * rq + 2], d[4 * rq + 3])});
    }
    stb64c(rs_ws, base + (3 + tl) * 336, v2u{pkbf(d[12], d[13]), pkbf(d[14], d[15])});
    drain();
    if (lane == 0) setflag(ws, WS_FLAG + (0 * 16 + J) * 512 + (P * 4 + wave) * 4, tag);
    STAMP(6);
  }
  WST(8);
  const int bq = sopq(b);
  const bool dog2 = bq < 216;
  const int g2c = bq >> 2, g2k = bq & 3;
  v4u gw[5], gq[4], gr[4];
  const int ge = lane < 40 ? lane : 39, gvec = ge >= 20;
  const u32 gof = K2 ? OOB : WS_MODG + ((wave == 1 ? 1280 : wave == 2 ? 3200 : 2560) + CT * 80 + 2 * ge) * 8;
  const u32 gor = K2 ? OOB : WS_MODG + (1920 + gvec * 640 + 160 * g2k + 40 * T4) * 8 + (ge - 20 * gvec) * 16;
  if (K2) {
    const u32 so = (128 * P + 64 * TH + 16 * T4) * 1280 + CT * 160;
#pragma unroll
    for (int k = 0; k < 3; ++k) dmam<60>(rs_x, v10, so + 5 * k * 1280, 32768 + T4 * 2560 + k * 800);
  } else {
    const rsrc_t rs_gu = mkrs(g2c < 27 ? wg : wu, DM * FF * 2);
    const u32 vo = dog2 ? (160 * g2k + 40 * T4 + (lane >> 3)) * 3456 + (g2c < 27 ? g2c : g2c - 27) * 128 + (lane & 7) * 16 : OOB;
#pragma unroll
    for (int i = 0; i < 5; ++i) gw[i] = ldb128(rs_gu, vo, i * 8 * 3456);
#pragma unroll
    for (int i = 0; i < 4; ++i) { gr[i] = ldb128c(rs_ws, gor, i * 30720); gq[i] = ldb128c(rs_ws, gof, i * 30720); }
  }
  SB();
  auto wo1 = [&](int e) { dma16c(rs_ws, vlin, WS_WO + J * 102400 + e * 1024, e * 1024); };
  u32 pq[10];
  {
    v4f aQ[3] = {};
    const int odd = R20 & 1;
    const u32 bq = opq(L_RQ + odd * 2560 + vlin), bl = opq(L_RQ + odd * 2560 + 2048 + g4 * 128 + ((t16 >> 3) * 4 + p4) * 16);
    const u32 bq19 = bq + (odd ? -2560 : 19 * 2560), bl19 = bl + (odd ? -2560 : 19 * 2560);
    auto qf = [&](int n, int t) { return asbf(ldsr128(n == 19 ? (t == 2 ? bl19 : bq19 + t * 1024) : (t == 2 ? bl + n * 2560 : bq + n * 2560 + t * 1024))); };
    v8bf wf[9];
#pragma unroll
    for (int f = 0; f < 9; ++f) wf[f] = qf(f / 3, f % 3);
    SB();
#pragma unroll
    for (int n = 0; n < 60; ++n) {
      aQ[n % 3] = mfma16(wf[n % 9], asbf(hf[n / 3]), aQ[n % 3]);
      if (n + 9 < 60) wf[n % 9] = qf((n + 9) / 3, n % 3);
      SB();
    }
    asm volatile("" : "+v"(aQ[0]), "+v"(aQ[1]), "+v"(aQ[2]));
    phi(aQ[0], aQ[1], aQ[2], pq);
#pragma unroll
    for (int i = 0; i < 10; ++i) asm volatile("" : "+v"(pq[i]));
  }
  WST(9);
  v2u xq[5];
  if (wave < 4) {
    if (!tags4(gr[0], gr[1], gr[2], gr[3], tag)) gather4(rs_ws, gor, 30720, tag, gr[0], gr[1], gr[2], gr[3]);
    if (!tags4(gq[0], gq[1], gq[2], gq[3], tag)) gather4(rs_ws, gof, 30720, tag, gq[0], gq[1], gq[2], gq[3]);
    *(LAS v2f*)(size_t)(L_G2A + T4 * 320 + ge * 8) = sum4(gr[0], gr[1], gr[2], gr[3], gvec ? 1.f : 0.f);
    if (wave) *(LAS v2f*)(size_t)(L_WPHI + (wave - 1) * 320 + ge * 8) = sum4(gq[0], gq[1], gq[2], gq[3], wave == 3 ? 1.f : 0.f);
    float sa[8] = {}, sb2[8] = {};
#pragma unroll
    for (int i = 0; i < 5; ++i) {
      const float cs = ldsrf(L_G2A + T4 * 320 + (8 * i + (lane >> 3)) * 4), ca = ldsrf(L_G2A + T4 * 320 + 160 + (8 * i + (lane >> 3)) * 4);
#pragma unroll
      for (int z = 0; z < 4; ++z) {
        const float lo = bflo(gw[i][z]), hi = bfhi(gw[i][z]);
        sa[2 * z] += ca * lo; sa[2 * z + 1] += ca * hi; sb2[2 * z] += cs * lo; sb2[2 * z + 1] += cs * hi;
      }
    }
    const u32 a = L_G2R + T4 * 512 + ((lane & 7) * 8 + 4 * hh + 2 * g2 + ((lane >> 3) & 1)) * 4;
    ldswf(a, red8(sa, lane)); ldswf(a + 256, red8(sb2, lane));
    lds_wait(L_SYNC + 48 + (lane & 3) * 4, 2u);
#pragma unroll 1
    for (int e = wave; e < WO_EARLY; e += 4) wo1(e);
  } else {
    drain();
#pragma unroll
    for (int t = 0; t < 5; ++t) { xq[t] = ldsr64(32768 + T4 * 2560 + t16 * 160 + t * 32 + g4 * 8); asm volatile("" : "+v"(xq[t])); }
    if (wave == 4) { poll_n(ws, WS_FLAG + (0 * 16 + J) * 512, 64, tag, lane); lds_post(L_SYNC + 28, 1u); } else lds_wait(L_SYNC + 28, 1u);
    const int t2 = tid - 256;
    v4u pv[16];
    const u32 c16 = t2 < 210 ? t2 * 16 : OOB;
#pragma unroll
    for (int i = 0; i < 16; ++i) pv[i] = ldb128c(rs_ws, c16, WS_KVP + (J * 16 + i) * KVP_STRIDE);
    lds_post(L_SYNC + 32 + wave * 4, 2u);
    u32 la[2];
#pragma unroll
    for (int z = 0; z < 2; ++z) { const int c = t2 < 210 ? 2 * t2 + z : 0, hd = c >= 210, rem = c - hd * 210, fq = rem / 21; la[z] = opq(L_KVB + hd * 1680 + (rem - fq * 21) * 80 + fq * 8); }
    SB();
    float sm[8] = {};
#pragma unroll
    for (int i = 0; i < 16; ++i)
#pragma unroll
      for (int e = 0; e < 4; ++e) { sm[2 * e] += bflo(pv[i][e]); sm[2 * e + 1] += bfhi(pv[i][e]); }
    if (t2 < 210) { ldsw64(la[0], v2u{pkbf(sm[0], sm[1]), pkbf(sm[2], sm[3])}); ldsw64(la[1], v2u{pkbf(sm[4], sm[5]), pkbf(sm[6], sm[7])}); }
  }
  STAMP(7); STAMP(8);
  WST(10);
  bar();
  STAMP(9);
  {
    const bool act = (t16 >> 3) == hh;
    const int hf2 = q4 & 1;
    const u32 kb = L_KVB + hh * 1680;
    const u32 a01 = kb + (4 * hh + 8 * hf2 + p4) * 80, a2 = kb + (hf2 ? 20 : (hh ? 0 : 16) + p4) * 80;
    v4f d[3] = {};
    const v4u b0 = {pq[0], pq[1], pq[2], pq[3]}, b1 = {pq[4], pq[5], pq[6], pq[7]}, b2 = {pq[8], pq[9], 0u, 0u};
#pragma unroll
    for (int T = 0; T < 3; ++T) {
      const u32 r = T < 2 ? a01 + T * 320 : a2;
      const v4u k0 = ldsr128(act ? r + g2 * 48 : L_ZERO), k1 = ldsr128(act ? r + 16 + g2 * 48 : L_ZERO);
      const v2u k2 = ldsr64(act ? r + 32 + g2 * 8 : L_ZERO);
      d[T] = mfma16(asbf(k0), asbf(b0), d[T]); d[T] = mfma16(asbf(k1), asbf(b1), d[T]); d[T] = mfma16(asbf(v4u{k2[0], k2[1], 0u, 0u}), asbf(b2), d[T]);
    }
    const auto dn = __builtin_amdgcn_permlane16_swap(__float_as_uint(d[2][0]), __float_as_uint(d[2][0]), false, false);
    const float rd = 1.0f / (__uint_as_float(dn[1]) + 1e-6f);
    const u32 ob = WS_O + ((8 * P + wave) * 80 + 5 * J) * 256 + t16 * 16;
    stb128c(rs_ws, ob + (3 * hh + g2) * 256, v4u{pkbf(d[0][0] * rd, d[0][1] * rd), pkbf(d[0][2] * rd, d[0][3] * rd), pkbf(d[1][0] * rd, d[1][1] * rd), pkbf(d[1][2] * rd, d[1][3] * rd)});
    stb64c(rs_ws, g2 ? OOB : ob + 512 + hh * 8, v2u{pkbf(d[2][0] * rd, d[2][1] * rd), pkbf(d[2][2] * rd, d[2][3] * rd)});
  }
  publish(ws, WS_FLAG + (1 * 16 + P) * 512 + J * 4, tag);
  STAMP(10);
  R20 = sopq(R20);
  const u32 gus = WS_GU + J * 286720;
  const int tokE = 128 * P + 64 * TH + 16 * T4 + t16;
  if (dog2 && wave == 7) {
    const u32 a = L_G2R + lane * 4;
    const float av = (ldsrf(a) + ldsrf(a + 512)) + (ldsrf(a + 1024) + ldsrf(a + 1536));
    const float bv = (ldsrf(a + 256) + ldsrf(a + 768)) + (ldsrf(a + 1280) + ldsrf(a + 1792));
    stb64c(rs_ws, WS_AB + (g2k * 3456 + g2c * 64 + lane) * 16, v2u{__float_as_uint(av), tag});
    stb64c(rs_ws, WS_AB + (g2k * 3456 + g2c * 64 + lane) * 16 + 8, v2u{__float_as_uint(bv), tag});
  }
  if (wave >= 1) {
#pragma unroll 1
    for (int e = WO_EARLY + wave - 1; e < 100; e += 7) wo1(e);
    drain();
  }
  SB();
  WST(11);
  if (wave == 0) poll_n(ws, WS_FLAG + (1 * 16 + P) * 512, 16, tag, lane);
  bar();
  STAMP(11);
  auto gu1 = [&](int e8) {
    const u32 src = e8 < 160 ? wrap(e8 + 8 * R20, 160) + wave : 160 + wrap(e8 - 160 + wave + 6 * R20, 120);
    dma16c(rs_ws, vlin, gus + src * 1024, ((e8 + 104) % 144 + wave) * 1024);
  };
  u32 kb2[3];
  kb2[0] = opq(vlin + K2 * 5120); kb2[1] = opq(vlin + K2 * 5120 + 51200); kb2[2] = opq(vlin + K2 * 5120 + 102400);
  auto frag2 = [&](u32 off) { return asbf(ldsr128(kb2[off / 51200] + off % 51200)); };
  float x1[20];
  {
    v4u of[10];
    const u32 ab = WS_O + ((8 * P + 4 * TH + T4) * 80 + 4 * K2) * 256 + vlin;
    sfor<0, 10>([&](auto S) { aloadc<(S.v & 1) * 2048>(of[S.v], rs_ws, ab, (S.v >> 1) * 4096); });
    sfor<0, 5>([&](auto I) { gu1(8 * I.v); });
    v8bf wf[10];
    sfor<0, 10>([&](auto F) { wf[F.v] = frag2((F.v / 5) * 10240 + (F.v % 5) * 1024); });
    v4f acc[5] = {};
    SB();
    sfor<0, 10>([&](auto S) {
      constexpr int s = S.v;
      await1<14 - s>(of[s]);
      sfor<0, 5>([&](auto T) {
        constexpr int t = T.v, f = (5 * s + t) % 10;
        acc[t] = mfma16(wf[f], asbf(of[s]), acc[t]);
        if constexpr (s < 8) wf[f] = frag2((s + 2) * 10240 + t * 1024);
        SB();
      });
    });
    asm volatile("" : "+v"(acc[0]), "+v"(acc[1]), "+v"(acc[2]), "+v"(acc[3]), "+v"(acc[4]));
    WST(12);
    bar();
    const u32 xb = 81920 + T4 * 5120 + vlin;
    if (K2 == 0) {
#pragma unroll
      for (int t = 0; t < 5; ++t) ldsw128f(xb + t * 1024, acc[t]);
    }
    v4f ga[5], A2[5];
#pragma unroll
    for (int t = 0; t < 5; ++t) { ga[t] = ldsr128f(L_WPHI + t * 64 + g4 * 16); A2[t] = ldsr128f(L_WPHI + 640 + t * 64 + g4 * 16); asm volatile("" : "+v"(ga[t]), "+v"(A2[t])); }
    const u32 uo = opq(WS_U + ((tokE >> 4) * 80 + CT * 10 + hh) * 256 + t16 * 16 + g2 * 8);
    bar();
    if (K2 == 1) {
      v4f s1 = {}, s2 = {};
#pragma unroll
      for (int t = 0; t < 5; ++t) {
        const v4f y = v4f{bflo(xq[t][0]), bfhi(xq[t][0]), bflo(xq[t][1]), bfhi(xq[t][1])} + ga[t] * (acc[t] + ldsr128f(xb + t * 1024));
        const u32 p0 = pkbf(y[0], y[1]), p1 = pkbf(y[2], y[3]);
        const v4f r = {bflo(p0), bfhi(p0), bflo(p1), bfhi(p1)}, u = r * A2[t];
        stb64c(rs_ws, uo + t * 512, v2u{pkbf(u[0], u[1]), pkbf(u[2], u[3])});
        x1[4 * t] = r[0]; x1[4 * t + 1] = r[1]; x1[4 * t + 2] = r[2]; x1[4 * t + 3] = r[3];
        s1 += r;
      }
      const float mt = xsum32(xsum16((s1[0] + s1[1]) + (s1[2] + s1[3]))) * (1.f / 80.f);
#pragma unroll
      for (int t = 0; t < 5; ++t) { const v4f d = v4f{x1[4 * t], x1[4 * t + 1], x1[4 * t + 2], x1[4 * t + 3]} - mt; s2 += d * d; }
      const float m2 = xsum32(xsum16((s2[0] + s2[1]) + (s2[2] + s2[3])));
      stb64c(rs_ws, g4 == 0 ? WS_STAT + ((P * 8 + CT) * 128 + (tokE & 127)) * 8 : OOB, v2u{__float_as_uint(mt), __float_as_uint(m2)});
      drain();
      if (lane == 0) setflag(ws, WS_FLAG + (2 * 16 + P) * 512 + (J * 4 + T4) * 4, tag);
#pragma unroll 1
      for (int z = 0; z < 6; ++z) { const int e = z < 5 ? 120 + 5 * T4 + z : 140 + T4; dma16c(rs_ws, vlin, gus + wrap(e + 8 * R20, 160) * 1024, (e - 40) * 1024); }
    } else {
#pragma unroll
      for (int i = 0; i < 20; ++i) x1[i] = 0.f;
      {
        const int abe = T4 * 56 + (lane < 56 ? lane : 55), mat = abe >= 112, c = abe - 112 * mat;
        v4u g0, g1, g2v, g3;
        gather4(rs_ws, WS_AB + (mat * 1728 + (t0 * 16 + c < 1728 ? t0 * 16 + c : 1727)) * 16, 3456 * 16, tag, g0, g1, g2v, g3);
        ldswf(L_AB + abe * 4, (__uint_as_float(g0[0]) + __uint_as_float(g1[0])) + (__uint_as_float(g2v[0]) + __uint_as_float(g3[0])));
        ldswf(L_AB + 896 + abe * 4, (__uint_as_float(g0[2]) + __uint_as_float(g1[2])) + (__uint_as_float(g2v[2]) + __uint_as_float(g3[2])));
      }
      if (wave > 0) {
#pragma unroll 1
        for (int e = 39 + wave; e < 120; e += 3) { dma16c(rs_ws, vlin, gus + wrap(e + 8 * R20, 160) * 1024, (e - 40) * 1024); waitv<GU_THR>(); }
      }
    }
  }
  STAMP(12);
  WST(13);
  const u32 wds = WS_WD + CT * 276480;
  v4u af[27];
  const u32 ab4 = WS_ACT + ((8 * P + 4 * TH + T4) * 216 + 4 * K2) * 256 + vlin;
  auto lda = [&](auto N) { constexpr int n = N.v; aloadc<0>(af[n], rs_ws, ab4, (n < 16 ? wrap(n + R4A, 16) : 16 + wrap(n - 16 + R4B, 11)) * 2048); };
  auto wflag = [&](int hop) { bflag(rs_ws, tag, WS_FLAG + (hop * 16 + P) * 512 + (wave * 16 + J) * 4); };
  auto wd1 = [&](int e) { dma16c(rs_ws, vlin, wds + (e < 160 ? wrap(e + 10 * R4A, 160) : 160 + wrap(e - 160 + 10 * R4B, 110)) * 1024, wrap(wrap(e + 100, 150), 150) * 1024); };
  {
    if (wave == 0) poll_n(ws, WS_FLAG + (2 * 16 + P) * 512, 64, tag, lane);
    bar();
    STAMP(13);
    R20 = sopq(R20);
    v4u uf[20], st;
    const u32 uvo = WS_U + (8 * P + wave) * 20480 + vlin;
    constexpr int U0 = 8;
    auto ldu = [&](auto N) { aloadc<0>(uf[N.v], rs_ws, uvo, wrap(N.v + R20, 20) * 1024); };
    sfor<0, U0>(ldu);
    const u32 stoff = WS_STAT + ((P * 8 + 2 * g4 + (lane & 1)) * 128 + 16 * wave + (t16 & ~1)) * 8;
    constexpr auto sl = [](int e) { return (u32)(((e + 104) % 144) * 1024); };
    v8bf wf[8];
    sfor<0, 8>([&](auto F) { wf[F.v] = frag(sl(F.v)); });
    v4f aA[8] = {}, aB[6] = {};
    constexpr auto nops = [](int m) { return (m < 13 ? 1 : 0) + (m >= 8 ? 1 : 0); };
    constexpr auto sum = [=](int a, int b2) { int c = 0; for (int m = a; m < b2; ++m) c += nops(m); return c; };
    SB();
    sfor<0, 20>([&](auto N) {
      constexpr int n = N.v;
      if constexpr (n == 2 || n == 8 || n == 13) { rawbar(); SB(); }
      if constexpr (n == 17) { await1<3>(st); rawbar(); SB(); }
      await1<(n < U0 ? U0 - 1 - n + sum(0, n) : nops(n - U0) - 1 + sum(n - U0 + 1, n))>(uf[n]);
      sfor<0, 8>([&](auto T) {
        constexpr int t = T.v;
        aA[t] = mfma16(wf[t], asbf(uf[n]), aA[t]);
        wf[t] = frag(sl(8 * n + 8 + t));
        if constexpr (t == 0) {
          if constexpr (n + U0 < 20) ldu(IC<(n + U0 < 20 ? n + U0 : 0)>{});
          else if constexpr (n == 12) aloadc<0>(st, rs_ws, stoff, 0);
        }
        if constexpr (t == 4 && n >= 8) gu1(144 + 8 * (n - 8));
        SB();
      });
    });
    asm volatile("" : "+v"(aA[0]), "+v"(aA[1]), "+v"(aA[2]), "+v"(aA[3]), "+v"(aA[4]), "+v"(aA[5]), "+v"(aA[6]), "+v"(aA[7]));
    STAMP(14);
    float rs2, mr2;
    {
      const bool od = lane & 1;
      const float ma = __uint_as_float(od ? st[2] : st[0]), qa = __uint_as_float(od ? st[3] : st[1]);
      const float mb = __uint_as_float(__builtin_amdgcn_update_dpp(0u, od ? st[0] : st[2], 0xb1, 0xf, 0xf, true));
      const float qb = __uint_as_float(__builtin_amdgcn_update_dpp(0u, od ? st[1] : st[3], 0xb1, 0xf, 0xf, true));
      const float m = xsum32(xsum16(ma + mb)) * 0.125f;
      const float q = qa + qb + 80.f * ((ma - m) * (ma - m) + (mb - m) * (mb - m));
      rs2 = __builtin_amdgcn_rsqf(xsum32(xsum16(q)) * (1.f / DM) + 1e-5f); mr2 = -m * rs2;
    }
    auto geglu4 = [&](const v4f& g, const v4f& u, u32 cb) {
      const v4f ag = ldsr128f(cb), au = ldsr128f(cb + 448), bg = ldsr128f(cb + 896), bu = ldsr128f(cb + 1344);
      const v4f gv = g * rs2 + (ag * mr2 + bg), uv = u * rs2 + (au * mr2 + bu);
      const v4f z = gv * (gv * gv * (-0.044715f * 2.302208198f) + (-2.302208198f));
      v4f d = {__builtin_amdgcn_exp2f(z[0]), __builtin_amdgcn_exp2f(z[1]), __builtin_amdgcn_exp2f(z[2]), __builtin_amdgcn_exp2f(z[3])};
      d += 1.f;
      const v4f r = {__builtin_amdgcn_rcpf(d[0]), __builtin_amdgcn_rcpf(d[1]), __builtin_amdgcn_rcpf(d[2]), __builtin_amdgcn_rcpf(d[3])};
      const v4f o = gv * uv * r;
      return v2u{pkbf(o[0], o[1]), pkbf(o[2], o[3])};
    };
    const u32 abm = opq(L_AB + g4 * 32);
    const u32 ob = WS_ACT + (8 * P + wave) * 55296;
    auto epiA = [&]() {
#pragma unroll
      for (int bk = 0; bk < 2; ++bk) {
        const v2u lo = geglu4(aA[4 * bk], aA[4 * bk + 2], abm + bk * 128), hi = geglu4(aA[4 * bk + 1], aA[4 * bk + 3], abm + bk * 128 + 16);
        astorec(v4u{lo[0], lo[1], hi[0], hi[1]}, rs_ws, vlin, ob + (2 * J + bk) * 1024);
      }
    };
    auto loopB = [&](auto N0, auto N1) {
      sfor<6 * N0.v, 6 * N1.v>([&](auto I) {
        constexpr int i = I.v, sb = i / 6;
        aB[i % 6] = mfma16(wf[i % 8], asbf(uf[sb]), aB[i % 6]);
        if constexpr (i + 8 < 120) wf[i % 8] = frag(sl(160 + i + 8));
        if constexpr (sb < 5 && i % 6 == 3) gu1(240 + 8 * sb);
        if constexpr (sb >= 5 && sb < 8 && i % 6 == 3) { if constexpr (sb < 7) wd1(8 * (sb - 5) + wave); else wd1(16 + (wave & 3)); }
        if constexpr (sb >= 10 && sb < 14 && i % 3 == 1) wd1(20 + 8 * ((i - 60) / 3) + wave);
        if constexpr (sb >= 15 && sb < 19 && i % 6 == 1) wd1(84 + 8 * (sb - 15) + wave);
        if constexpr (sb >= 15 && i % 6 == 4) lda(IC<(sb >= 15 ? sb - 15 : 0)>{});
        SB();
      });
    };
    auto pinB = [&]() { asm volatile("" : "+v"(aB[0]), "+v"(aB[1]), "+v"(aB[2]), "+v"(aB[3]), "+v"(aB[4]), "+v"(aB[5])); };
    epiA();
    SB();
    loopB(IC<0>{}, IC<4>{});
    waitv<6>();
    rawbar();
    loopB(IC<4>{}, IC<7>{});
    waitv<7>();
    wflag(3);
    loopB(IC<7>{}, IC<10>{});
    waitv<4>();
    rawbar();
    STAMP(15);
    loopB(IC<10>{}, IC<15>{});
    pinB();
    if (wave == 0) poll_n(ws, WS_FLAG + (3 * 16 + P) * 512 + TH * 256, 64, tag, lane);
    rawbar();
    loopB(IC<15>{}, IC<20>{});
    pinB();
    STAMP(16);
    {
      const v2u lo = geglu4(aB[0], aB[2], abm + 256), hi = geglu4(aB[1], aB[3], abm + 272);
      astorec(v4u{lo[0], lo[1], hi[0], hi[1]}, rs_ws, vlin, ob + (32 + J) * 1024);
      const v2u hf4 = geglu4(aB[4], aB[5], L_AB + 384 + g4 * 16);
      __builtin_amdgcn_raw_buffer_store_b64(hf4, rs_ws, nt == 7 ? hh * 256 + t16 * 16 + g2 * 8 : OOB, ob + 48 * 1024 + (J - (J >> 2)) * 512, AUX_SC1);
    }
  }
  WST(14);
  sfor<5, 10>(lda);
  await4<7>(af[0], af[1], af[2], af[3]);
  asm volatile("" : "+v"(af[4]));
  rawbar();
  STAMP(17);
  {
    constexpr auto sl = [](int n, int t) { return (u32)(((n + 10) % 15) * 10240 + t * 1024); };
    v8bf wf[10];
    sfor<0, 10>([&](auto F) { wf[F.v] = frag2(sl(F.v / 5, F.v % 5)); });
    v4f acc[5] = {};
    constexpr auto nops = [](int m) { return ((m < 6 || (m >= 8 && m <= 18)) ? 1 : 0) + (m < 20 ? 1 : 0) + (m == 2 ? 1 : 0); };
    constexpr auto sum = [=](int a, int b2) { int c = 0; for (int m = a; m < b2; ++m) c += nops(m); return c; };
    SB();
    sfor<0, 27>([&](auto N) {
      constexpr int n = N.v;
      if constexpr (n == 2) { waitv<5 + sum(0, 2)>(); wflag(4); }
      if constexpr (n == 8) { if (wave == 0) poll_n(ws, WS_FLAG + (4 * 16 + P) * 512 + TH * 256, 64, tag, lane); }
      if constexpr (n >= 4 && n <= 24 && (n & 1) == 0) { if constexpr (n >= 8) waitv<sum(n - 4, n)>(); rawbar(); SB(); }
      if constexpr (n >= 5 && n < 10) await1<9 - n + sum(0, n)>(af[n]);
      if constexpr (n >= 10 && n < 16) await1<1 + sum(n - 9, n)>(af[n]);
      if constexpr (n >= 16) await1<1 + sum(n - 7, n)>(af[n]);
      sfor<0, 5>([&](auto T) {
        constexpr int t = T.v, f = (5 * n + t) % 10;
        acc[t] = mfma16(wf[f], asbf(af[n]), acc[t]);
        if constexpr (n < 25) wf[f] = frag2(sl(n + 2, t));
        if constexpr (n < 6 && t == 0) lda(IC<(n < 6 ? n + 10 : 0)>{});
        if constexpr (n >= 8 && n <= 18 && t == 0) lda(IC<(n <= 18 ? n + 8 : 0)>{});
        if constexpr (n < 20 && t == 2) { const int e = 116 + 8 * n + wave; wd1(n == 19 ? (e < 270 ? e : 269) : e); }
        SB();
      });
      if constexpr (n == 7) asm volatile("" : "+v"(acc[0]), "+v"(acc[1]), "+v"(acc[2]), "+v"(acc[3]), "+v"(acc[4]));
    });
    asm volatile("" : "+v"(acc[0]), "+v"(acc[1]), "+v"(acc[2]), "+v"(acc[3]), "+v"(acc[4]));
    STAMP(18);
    const u32 xb = 81920 + T4 * 5120 + vlin;
    if (K2 == 0) {
#pragma unroll
      for (int t = 0; t < 5; ++t) ldsw128f(xb + t * 1024, acc[t]);
    }
    bar();
    if (K2 == 1) {
      const u32 ob = 102400 + T4 * 2816;
#pragma unroll
      for (int t = 0; t < 5; ++t) {
        const v4f a = acc[t] + ldsr128f(xb + t * 1024);
        const v4f gb = ldsr128f(L_WPHI + 320 + t * 64 + g4 * 16);
        ldsw64(ob + t16 * 176 + t * 32 + g4 * 8, v2u{pkbf(x1[4 * t] + gb[0] * a[0], x1[4 * t + 1] + gb[1] * a[1]), pkbf(x1[4 * t + 2] + gb[2] * a[2], x1[4 * t + 3] + gb[3] * a[3])});
      }
#pragma unroll
      for (int i = 0; i < 3; ++i) {
        const int it = i * 64 + lane, tk = it / 10, ch = it - tk * 10;
        if (it < 160) *(GAS v4u*)((GAS char*)out + (128 * P + 64 * TH + 16 * T4 + tk) * 1280 + CT * 160 + ch * 16) = ldsr128(ob + tk * 176 + ch * 16);
      }
    }
  }
  if (b == 0 && tid == 0) __hip_atomic_store((GAS u32*)(ws + WS_EPOCH), tag, RLX_AGENT);
  WST(15);
  STAMP(19);
}
}

extern "C" void kernel_launch(void* const* d_in, const int* in_sizes, int n_in, void* d_out, int out_size, void* d_ws, size_t ws_size, hipStream_t stream) {
  (void)in_sizes; (void)n_in; (void)out_size; (void)ws_size;
  (void)hipFuncSetAttribute((const void*)fused_kernel, hipFuncAttributeMaxDynamicSharedMemorySize, L_TOTAL);
  fused_kernel<<<256, 512, L_TOTAL, stream>>>((const u16*)d_in[0], (const u16*)d_in[1], (const u16*)d_in[2], (const u16*)d_in[3], (const u16*)d_in[5],
                                              (const u16*)d_in[6], (const u16*)d_in[7], (const u16*)d_in[8], (const u16*)d_in[9], (u16*)d_out, (char*)d_ws);
}
```

```cpp
#include <hip/hip_runtime.h>
#include <stdint.h>

typedef unsigned short u16;
typedef unsigned u32;
typedef unsigned long long u64;
typedef u32 v4u __attribute__((ext_vector_type(4)));
typedef u32 v2u __attribute__((ext_vector_type(2)));
typedef float v4f __attribute__((ext_vector_type(4)));
typedef float v2f __attribute__((ext_vector_type(2)));
typedef float v16f __attribute__((ext_vector_type(16)));
typedef short v4s __attribute__((ext_vector_type(4)));
typedef __bf16 v8bf __attribute__((ext_vector_type(8)));
typedef __bf16 v2bf __attribute__((ext_vector_type(2)));
#define LAS __attribute__((address_space(3)))
#define GAS __attribute__((address_space(1)))
#define DI __device__ __forceinline__
#define RLX_AGENT __ATOMIC_RELAXED, __HIP_MEMORY_SCOPE_AGENT
#define SB() __builtin_amdgcn_sched_barrier(0)
typedef __amdgpu_buffer_rsrc_t rsrc_t;

namespace {
constexpr int SEQ = 2048, DM = 640, FF = 1728;
constexpr u32 WS_EPOCH = 0;
constexpr u32 WS_FLAG = 1024;
constexpr u32 WS_ST1 = 69632;
constexpr u32 WS_MODG = WS_ST1 + 32768;
constexpr u32 WS_AB = WS_MODG + 4 * 30720;
constexpr u32 WS_KVP = WS_AB + 4 * 3456 * 16;
constexpr u32 KVP_STRIDE = 3456;
constexpr u32 WS_STAT = WS_KVP + 256 * KVP_STRIDE;
constexpr u32 WS_O = WS_STAT + SEQ * 64;
constexpr u32 WS_U = WS_O + SEQ * DM * 2;
constexpr u32 WS_ACT = WS_U + SEQ * DM * 2;
constexpr u32 WS_GU = WS_ACT + SEQ * FF * 2;
constexpr u32 WS_WD = WS_GU + 16 * 286720;
constexpr u32 WS_WQ = WS_WD + 8 * 276480;
constexpr u32 WS_WO = WS_WQ + 16 * 153600;
constexpr u32 WS_T = WS_WO + 16 * 102400;
constexpr u32 WS_END = WS_T + SEQ * DM * 2;
static_assert(WS_KVP % 128 == 0 && WS_STAT % 128 == 0 && WS_O % 256 == 0, "align");
constexpr u32 L_RQ = 102400;
constexpr u32 L_RAW = 102400;
constexpr u32 L_MISC = 153600;
constexpr u32 L_COA = L_MISC, L_COS = L_MISC + 2560;
constexpr u32 L_KVB = L_MISC + 5120;
constexpr u32 L_ZERO = L_KVB + 3360;
constexpr u32 L_WPHI = L_ZERO + 16;
constexpr u32 L_SYNC = L_WPHI + 1600;
constexpr u32 L_TOTAL = 163840;
constexpr u32 L_G2A = L_COA;
constexpr u32 L_G2R = L_COA + 1280;
constexpr u32 L_AB = L_COA + 3328;
static_assert(L_SYNC + 72 <= L_TOTAL && L_AB + 1792 <= L_KVB, "lds");
constexpr int WO_EARLY = 48, GU_THR = 12;
#define CODE_PF 45056
constexpr u32 SPIN_CAP = 1u << 15;
constexpr int AUX_SC1 = 16;
constexpr u32 OOB = 0x7ffffff0u;

DI float bflo(u32 v) { return __uint_as_float(v << 16); }
DI float bfhi(u32 v) { return __uint_as_float(v & 0xffff0000u); }
DI u32 pkbf(float a, float b) { v2f t = {a, b}; return __builtin_bit_cast(u32, __builtin_convertvector(t, v2bf)); }
DI float rbf(float a) { return bflo(pkbf(a, a)); }
DI void bar() { asm volatile("s_waitcnt lgkmcnt(0)\n\ts_barrier" ::: "memory"); }
DI void rawbar() { asm volatile("s_barrier" ::: "memory"); }
DI void drain() { asm volatile("s_waitcnt vmcnt(0)" ::: "memory"); }
template <int N> DI void waitv() { asm volatile("s_waitcnt vmcnt(%0)" ::"i"(N) : "memory"); }
template <int I> struct IC { static constexpr int v = I; };
template <int B, int E, class F> DI void sfor(F&& f) { if constexpr (B < E) { f(IC<B>{}); sfor<B + 1, E>(f); } }
DI rsrc_t mkrs(const void* p, u32 bytes) { return __builtin_amdgcn_make_buffer_rsrc((void*)p, 0, bytes, 0x00020000); }
template <int NL> DI void dmam(rsrc_t rs, u32 voff, u32 soff, u32 lds) {
  asm volatile("s_mov_b32 m0, %0\n\ts_mov_b32 exec_hi, %4\n\tbuffer_load_dwordx4 %1, %2, %3 offen lds\n\ts_mov_b32 exec_hi, -1"
               ::"s"(lds), "v"(voff), "s"(rs), "s"(soff), "i"((1u << (NL - 32)) - 1u) : "memory");
}
DI void dma16c(rsrc_t rs, u32 voff, u32 soff, u32 lds) {
  asm volatile("s_mov_b32 m0, %0\n\ts_nop 0\n\tbuffer_load_dwordx4 %1, %2, %3 offen sc1 lds" ::"s"(lds), "v"(voff), "s"(rs), "s"(soff) : "memory");
}
DI u32 opq(u32 v) { asm volatile("" : "+v"(v)); return v; }
DI v4s trd(u32 addr) { return __builtin_amdgcn_ds_read_tr16_b64_v4i16((LAS v4s*)(size_t)addr); }
DI v8bf trfrag(u32 a0, u32 a1) {
  v4s a = trd(a0), b = trd(a1);
  return __builtin_bit_cast(v8bf, __builtin_shufflevector(a, b, 0, 1, 2, 3, 4, 5, 6, 7));
}
DI v16f mfma32(v8bf a, v8bf b, v16f c) { return __builtin_amdgcn_mfma_f32_32x32x16_bf16(a, b, c, 0, 0, 0); }
DI v4f mfma16(v8bf a, v8bf b, v4f c) { return __builtin_amdgcn_mfma_f32_16x16x32_bf16(a, b, c, 0, 0, 0); }
DI v8bf asbf(v4u v) { return __builtin_bit_cast(v8bf, v); }
DI v4u ldsr128(u32 a) { return *(LAS v4u*)(size_t)a; }
DI v4f ldsr128f(u32 a) { return *(LAS v4f*)(size_t)a; }
DI v2u ldsr64(u32 a) { return *(LAS v2u*)(size_t)a; }
DI void ldsw128(u32 a, v4u v) { *(LAS v4u*)(size_t)a = v; }
DI void ldsw128f(u32 a, v4f v) { *(LAS v4f*)(size_t)a = v; }
DI void ldsw64(u32 a, v2u v) { *(LAS v2u*)(size_t)a = v; }
DI float ldsrf(u32 a) { return *(LAS float*)(size_t)a; }
DI void ldswf(u32 a, float v) { *(LAS float*)(size_t)a = v; }
DI float dot2(u32 a, u32 b, float c) { return __builtin_amdgcn_fdot2_f32_bf16(__builtin_bit_cast(v2bf, a), __builtin_bit_cast(v2bf, b), c, false); }
DI float xsum16(float v) {
  auto r = __builtin_amdgcn_permlane16_swap(__float_as_uint(v), __float_as_uint(v), false, false);
  return __uint_as_float(r[0]) + __uint_as_float(r[1]);
}
DI float xsum32(float v) {
  auto r = __builtin_amdgcn_permlane32_swap(__float_as_uint(v), __float_as_uint(v), false, false);
  return __uint_as_float(r[0]) + __uint_as_float(r[1]);
}
template <int CTRL> DI float dppadd(float v) {
  return v + __uint_as_float(__builtin_amdgcn_update_dpp(0u, __float_as_uint(v), CTRL, 0xf, 0xf, true));
}
constexpr int DPP_ROR4 = 0x124, DPP_ROR8 = 0x128;
DI float red8(const float (&a)[8], int lane) {
  float b4[4], c[2];
#pragma unroll
  for (int e = 0; e < 4; ++e) { auto r = __builtin_amdgcn_permlane32_swap(__float_as_uint(a[e]), __float_as_uint(a[4 + e]), false, false); b4[e] = __uint_as_float(r[0]) + __uint_as_float(r[1]); }
#pragma unroll
  for (int e = 0; e < 2; ++e) { auto r = __builtin_amdgcn_permlane16_swap(__float_as_uint(b4[e]), __float_as_uint(b4[2 + e]), false, false); c[e] = __uint_as_float(r[0]) + __uint_as_float(r[1]); }
  const float x = dppadd<DPP_ROR8>(c[0]), y = dppadd<DPP_ROR8>(c[1]);
  return (lane & 8) ? y : x;
}
DI v4u ldb128(rsrc_t rs, u32 vo, u32 so) { return __builtin_amdgcn_raw_buffer_load_b128(rs, vo, so, 0); }
DI v4u ldb128c(rsrc_t rs, u32 vo, u32 so) { return __builtin_amdgcn_raw_buffer_load_b128(rs, vo, so, AUX_SC1); }
template <int OFF> DI void aload(v4u& r, rsrc_t rs, u32 vo, u32 so) {
  asm volatile("buffer_load_dwordx4 %0, %1, %2, %3 offen offset:%4" : "=v"(r) : "v"(vo), "s"(rs), "s"(so), "i"(OFF) : "memory");
}
template <int OFF> DI void aloadc(v4u& r, rsrc_t rs, u32 vo, u32 so) {
  asm volatile("buffer_load_dwordx4 %0, %1, %2, %3 offen offset:%4 sc1" : "=v"(r) : "v"(vo), "s"(rs), "s"(so), "i"(OFF) : "memory");
}
template <int N> DI void await1(v4u& a) { asm volatile("s_waitcnt vmcnt(%1)" : "+v"(a) : "i"(N) : "memory"); }
DI void astorec(v4u v, rsrc_t rs, u32 vo, u32 so) { u32 o = vo + so; asm volatile("" : "+v"(o)); __builtin_amdgcn_raw_buffer_store_b128(v, rs, o, 0, AUX_SC1); }
DI int sopq(int v) { asm volatile("" : "+s"(v)); return v; }
DI void stb128c(rsrc_t rs, u32 vo, v4u v) { __builtin_amdgcn_raw_buffer_store_b128(v, rs, vo, 0, AUX_SC1); }
DI void stb64c(rsrc_t rs, u32 vo, v2u v) { __builtin_amdgcn_raw_buffer_store_b64(v, rs, vo, 0, AUX_SC1); }

DI void setflag(char* ws, u32 off, u32 tag) { (void)__hip_atomic_exchange((GAS u32*)(ws + off), tag, RLX_AGENT); }
DI void bflag(rsrc_t rs, u32 tag, u32 off) { asm volatile("s_mov_b64 exec, 1\n\tbuffer_atomic_swap %0, off, %2, %1\n\ts_mov_b64 exec, -1" ::"v"(tag), "s"(off), "s"(rs) : "memory"); }
DI void poll_n(char* ws, u32 off, int n, u32 tag, int lane) {
  GAS u32* f = (GAS u32*)(ws + off) + lane;
#pragma unroll 1
  for (u32 spin = 0; spin < SPIN_CAP; ++spin) {
    u32 v = tag;
    if (lane < n) { v = 0; asm volatile("global_atomic_or %0, %1, %0, off sc0 sc1\n\ts_waitcnt vmcnt(0)" : "+v"(v) : "v"(f) : "memory"); }
    if (__all(v == tag)) break;
    __builtin_amdgcn_s_sleep(1);
  }
}
DI void lds_post(u32 a, u32 tag) { asm volatile("" ::: "memory"); *(volatile LAS u32*)(size_t)a = tag; }
DI void lds_wait(u32 a, u32 tag) {
#pragma unroll 1
  for (u32 spin = 0; spin < (SPIN_CAP << 4); ++spin) {
    if (__all(*(volatile LAS u32*)(size_t)a >= tag)) break;
    __builtin_amdgcn_s_sleep(2);
  }
  asm volatile("" ::: "memory");
}
DI void gather2(rsrc_t rs, u32 o1, u32 o2, u32 tag, v4u& g1, v4u& g2) {
#pragma unroll 1
  for (u32 spin = 0; spin < SPIN_CAP; ++spin) {
    asm volatile("" ::: "memory");
    g1 = ldb128c(rs, o1, 0);
    g2 = ldb128c(rs, o2, 0);
    if (__all(g1[1] == tag && g1[3] == tag && g2[1] == tag && g2[3] == tag)) break;
    __builtin_amdgcn_s_sleep(1);
  }
}
DI void gather4(rsrc_t rs, u32 o, u32 st, u32 tag, v4u& g0, v4u& g1, v4u& g2, v4u& g3) {
#pragma unroll 1
  for (u32 spin = 0; spin < SPIN_CAP; ++spin) {
    asm volatile("" ::: "memory");
    g0 = ldb128c(rs, o, 0); g1 = ldb128c(rs, o, st); g2 = ldb128c(rs, o, 2 * st); g3 = ldb128c(rs, o, 3 * st);
    if (__all(g0[1] == tag && g0[3] == tag && g1[1] == tag && g1[3] == tag && g2[1] == tag && g2[3] == tag && g3[1] == tag && g3[3] == tag)) break;
    __builtin_amdgcn_s_sleep(1);
  }
}
DI v2f sum4(const v4u& a, const v4u& b, const v4u& c, const v4u& d, float one) {
  const float x = (__uint_as_float(a[0]) + __uint_as_float(b[0])) + (__uint_as_float(c[0]) + __uint_as_float(d[0]));
  const float y = (__uint_as_float(a[2]) + __uint_as_float(b[2])) + (__uint_as_float(c[2]) + __uint_as_float(d[2]));
  return v2f{rbf(one + rbf(x)), rbf(one + rbf(y))};
}
DI bool tags4(const v4u& a, const v4u& b, const v4u& c, const v4u& d, u32 t) {
  return __all(a[1] == t && a[3] == t && b[1] == t && b[3] == t && c[1] == t && c[3] == t && d[1] == t && d[3] == t);
}

DI void softmax20(const float (&p)[20], u32 (&o)[10]) {
  float mx = p[0];
#pragma unroll
  for (int i = 1; i < 20; ++i) mx = fmaxf(mx, p[i]);
  const float L2E = 1.4426950408889634f, a = -mx * L2E;
  float e[20], s0 = 0.f, s1 = 0.f;
#pragma unroll
  for (int i = 0; i < 20; ++i) { e[i] = __builtin_amdgcn_exp2f(__builtin_fmaf(p[i], L2E, a)); if (i & 1) s1 += e[i]; else s0 += e[i]; }
  const float r = __builtin_amdgcn_rcpf(s0 + s1);
#pragma unroll
  for (int i = 0; i < 10; ++i) o[i] = pkbf(e[2 * i] * r, e[2 * i + 1] * r);
}
__global__ __launch_bounds__(512, 2) void fused_kernel(const u16* __restrict__ x, const u16* __restrict__ wqkv, const u16* __restrict__ wphi,
                                                       const u16* __restrict__ wo, const u16* __restrict__ wg, const u16* __restrict__ wu,
                                                       const u16* __restrict__ wd, const u16* __restrict__ cvec, const u16* __restrict__ wmod,
                                                       u16* __restrict__ out, char* ws) {
  const int tid = threadIdx.x, lane = tid & 63;
  const int wave = __builtin_amdgcn_readfirstlane(tid >> 6);
  const int b = blockIdx.x, xcd = b & 7, idx = b >> 3;
  __builtin_assume(wave >= 0 && wave < 8);
  __builtin_assume(b >= 0 && b < 256);
  const rsrc_t rs_cd = mkrs((const void*)(__builtin_amdgcn_s_getpc() & ~(u64)127), CODE_PF);
  auto codepf = [&]() {
    asm volatile("s_mov_b32 m0, %3\n\ts_mov_b64 exec, 3\n\tbuffer_load_dword %0, %1, %2 offen lds\n\ts_mov_b64 exec, -1" ::"v"(lane * 32768), "s"(rs_cd), "s"((wave * 32 + idx) * 128), "i"(L_SYNC + 64) : "memory");
  };
  codepf();
  SB();
  const int T4 = wave & 3, K2 = wave >> 2;
  const int mc = b >> 2, mk = b & 3;
  const u32 coff2 = (160 * mk + 40 * T4 + 5 * (lane >> 3)) * 2;
  const int P = 4 * (xcd >> 1) + (idx >> 3);
  const int J = 8 * (xcd & 1) + (idx & 7);
  const int TH = J >> 3, CT = J & 7;
  const rsrc_t rs_ws = mkrs(ws, WS_END);
  const rsrc_t rs_x = mkrs(x, SEQ * DM * 2);
  const u32 vlin = lane * 16;
  const int hh = lane >> 5, i32 = lane & 31, g4 = lane >> 4, t16 = lane & 15;
  const int g2 = g4 & 1, q4 = t16 >> 2, p4 = lane & 3;
  v4u wm[5], cq, tagq;
  {
    const rsrc_t rs_a = mkrs(K2 ? x : wmod, K2 ? SEQ * DM * 2 : DM * 6 * DM * 2), rs_c = mkrs(cvec, DM * 2);
    const u32 vo = K2 ? (128 * P + 8 * J + 2 * T4 + hh) * 1280 + i32 * 16 : coff2 * 3840 + mc * 128 + (lane & 7) * 16;
    aload<0>(cq, rs_c, coff2 & ~3u, 0);
    sfor<0, 5>([&](auto I) { aload<0>(wm[I.v], rs_a, vo, K2 ? (I.v < 3 ? I.v * 512 : 0x10000000) : I.v * 7680); });
    aloadc<0>(tagq, rs_ws, WS_EPOCH, 0);
  }
  SB();
  ldsw128(L_SYNC + (lane & 3) * 16, v4u{0u, 0u, 0u, 0u});
  ldsw128(L_ZERO, v4u{0u, 0u, 0u, 0u});
  auto wrap = [](int v, int m) { return v >= m ? v - m : v; };
  const int t0 = 7 * J - (J >> 2), nt = (J & 3) == 3 ? 6 : 7;
  const int rq27 = 2 * P + TH;
  const u32 wqs = WS_WQ + J * 153600;
  auto tile = [&](u32 src, u32 rstride, u32 dst, u32 so) {
    astorec(__builtin_bit_cast(v4u, trfrag(src + q4 * rstride, src + (4 + q4) * rstride)), rs_ws, dst, so);
  };
  const int l10 = lane / 10, c10 = lane - l10 * 10;
  const u32 v10 = l10 * 1280 + c10 * 16;
  constexpr u32 L_R2 = L_RAW + 11520;
  constexpr u32 L_RD = L_R2 + 17920;
  auto rawloads = [&]() {
    const rsrc_t rs_d = mkrs(wd, FF * DM * 2);
    const int row = lane / 14; int ch = lane - row * 14; if (ch >= 2 * nt) ch -= 2;
    const u32 vg = row * 3456 + ch * 16;
#pragma unroll 1
    for (int m = 0; m < 2; ++m) {
      const rsrc_t rs_g = mkrs(m ? wu : wg, DM * FF * 2);
#pragma unroll 1
      for (int i = 0; i < 10; ++i) dmam<56>(rs_g, vg, (40 * P + 4 * i) * 3456 + t0 * 32, L_R2 + m * 8960 + i * 896);
    }
    if (rq27 < 27) {
#pragma unroll 1
      for (int z = 0; z < 4; ++z) {
        const int o = 8 * rq27 + 2 * z;
        int jj, oo;
        if (o < 128) { jj = o >> 3; oo = o & 7; } else if (o < 192) { jj = (o - 128) >> 2; oo = 8 + (o & 3); } else { const int hq = (o - 192) >> 1; jj = hq + hq / 3; oo = 12; }
        const int wdrow = (7 * jj - (jj >> 2)) * 16 + oo * 8;
#pragma unroll 1
        for (int i = 0; i < 4; ++i) dmam<40>(rs_d, v10, (wdrow + 4 * i) * 1280 + CT * 160, L_RD + z * 2560 + i * 640);
      }
    }
  };
  constexpr u32 L_WR = 81920, L_OR = 92160;
  u32 ta[2] = {}, td[2][2] = {};
  if (K2) {
    {
      const rsrc_t rs_w = mkrs(wqkv, DM * 3 * DM * 2), rs_2 = mkrs(T4 == 1 ? wphi : wqkv, T4 == 1 ? 32 * 400 * 2 : DM * 3 * DM * 2);
      const int r = lane / 15, c = lane - r * 15, pt = c / 5;
      const u32 vo = r * 3840 + pt * 1280 + (c - pt * 5) * 16, so = (40 * P + 8 * T4) * 3840 + J * 80;
      dmam<60>(rs_w, vo, so, L_WR + T4 * 1920); dmam<60>(rs_w, vo, so + 4 * 3840, L_WR + T4 * 1920 + 960);
      const u32 v2 = T4 == 1 ? vlin : vo, s2 = T4 == 1 ? J * 1600 : (40 * P + 32) * 3840 + J * 80;
      dmam<60>(rs_2, v2, s2, T4 == 1 ? L_WPHI : L_WR + 7680); dmam<60>(rs_2, v2, s2 + (T4 == 1 ? 640 : 4 * 3840), T4 == 1 ? L_WPHI + 640 : L_WR + 8640);
    }
  {
    const u32 cm = (p4 >> 1) * 40 + (p4 & 1) * 16;
    ta[0] = opq(L_WR + q4 * 240 + 80 + hh * 80 + cm + g2 * 8);
    ta[1] = opq(L_WR + q4 * 240 + (g4 == 0 ? 80 + (p4 & 1) * 80 + (p4 >> 1) * 40 + 32 : g4 == 3 ? (p4 & 1) * 40 + 32 : cm + (g4 - 1) * 8));
    const u32 sA = g4 == 0 ? 5120 : 2560, sB = g4 == 3 ? 128 : 256, sC = (g4 == 0 ? 4096 : (g4 == 3 && t16 >= 8) ? OOB : 102400 + (g4 - 1) * 1024) + t16 * 16;
#pragma unroll
    for (int z = 0; z < 2; ++z) {
      const int oct = 5 * P + (z ? 4 : T4);
      td[z][0] = opq(wqs + (oct >> 2) * 5120 + (oct & 3) * 256 + g4 * 1024 + t16 * 16);
      td[z][1] = opq(wqs + (oct >> 2) * sA + (oct & 3) * sB + sC);
    }
  }
  } else {
    {
      const int r5 = lane / 5;
      const u32 vs = r5 * 1280 + (lane - r5 * 5) * 16, so = (128 * P + 32 * T4) * 1280 + J * 80, lx = L_RAW + T4 * 2880;
      dmam<60>(rs_x, vs, so, lx); dmam<60>(rs_x, vs, so + 12 * 1280, lx + 960); dmam<40>(rs_x, vs, so + 24 * 1280, lx + 1920);
    }
    codepf();
  }
  u32 tag;
  bar();
  await1<10>(cq);
  float sv[5];
  {
    const u32 sh = (coff2 & 2) * 8;
    const u32 ce[3] = {__builtin_amdgcn_alignbit(cq[1], cq[0], sh), __builtin_amdgcn_alignbit(cq[2], cq[1], sh), cq[2] >> sh};
#pragma unroll
    for (int i = 0; i < 5; ++i) {
      const float cv = (i & 1) ? bfhi(ce[i >> 1]) : bflo(ce[i >> 1]);
      sv[i] = rbf(cv * __builtin_amdgcn_rcpf(1.f + __builtin_amdgcn_exp2f(-1.4426950408889634f * cv)));
      asm volatile("" : "+v"(sv[i]));
    }
  }
  await1<4>(tagq);
  asm volatile("" : "+v"(wm[0]), "+v"(wm[1]), "+v"(wm[2]), "+v"(wm[3]), "+v"(wm[4]));
  tag = tagq[0] + 1u;
  if (!K2) {
    float acc[8] = {0.f, 0.f, 0.f, 0.f, 0.f, 0.f, 0.f, 0.f};
#pragma unroll
    for (int i = 0; i < 5; ++i)
#pragma unroll
      for (int e = 0; e < 4; ++e) { acc[2 * e] += sv[i] * bflo(wm[i][e]); acc[2 * e + 1] += sv[i] * bfhi(wm[i][e]); }
    ldswf(L_KVB + T4 * 256 + ((lane & 7) * 8 + 4 * hh + 2 * g2 + ((lane >> 3) & 1)) * 4, red8(acc, lane));
    if (wave) lds_post(L_SYNC + 12 + wave * 4, 1u);
    else {
      lds_wait(L_SYNC + 16 + (lane % 3) * 4, 1u);
      const u32 a = L_KVB + lane * 4;
      const float v = (ldsrf(a) + ldsrf(a + 256)) + (ldsrf(a + 512) + ldsrf(a + 768));
      stb64c(rs_ws, b < 240 ? WS_MODG + (mk * 3840 + mc * 64 + lane) * 8 : OOB, v2u{__float_as_uint(v), tag});
      const int vec = mc >= 10;
      if (mc < 20 && lane < 32 && ((40 * t16 + 39 * g2) >> 6) == mc - 10 * vec) setflag(ws, WS_FLAG + (6 * 16 + t16) * 512 + 256 + (vec * 8 + g2 * 4 + mk) * 4, tag);
    }
    if (wave == 1) rawloads();
    if (wave == 2) {
      const int e = lane < 40 ? lane : 39, vec = e >= 20;
      v4u g0, g1, g2v, g3;
      poll_n(ws, WS_FLAG + (6 * 16 + J) * 512 + 256, 16, tag, lane);
      gather4(rs_ws, WS_MODG + (vec * 640 + 40 * J) * 8 + (e - 20 * vec) * 16, 30720, tag, g0, g1, g2v, g3);
      *(LAS v2f*)(size_t)(L_COA + e * 8) = sum4(g0, g1, g2v, g3, vec ? 1.f : 0.f);
      lds_post(L_SYNC, 1u);
    } else if (wave == 3) {
      v4u g0, g1;
      poll_n(ws, WS_FLAG + (5 * 16 + P) * 512 + 256, 64, tag, lane);
      gather2(rs_ws, WS_ST1 + P * 2048 + vlin, WS_ST1 + P * 2048 + 1024 + vlin, tag, g0, g1);
      ldsw64(L_COS + lane * 8, v2u{g0[0], g0[2]}); ldsw64(L_COS + 512 + lane * 8, v2u{g1[0], g1[2]});
      lds_post(L_SYNC + 4, 1u);
    }
    drain();
    v4u xs[3];
    xs[0] = ldsr128(L_RAW + T4 * 2880 + t16 * 80 + g4 * 16); xs[1] = ldsr128(L_RAW + T4 * 2880 + 1280 + t16 * 80 + g4 * 16); xs[2] = ldsr128(L_RAW + T4 * 2880 + i32 * 80 + 64);
    lds_wait(L_SYNC + (lane & 1) * 4, 1u);
    const u32 cw = L_COA;
    const v2u s0 = ldsr64(L_COS + (32 * T4 + t16) * 8), s1 = ldsr64(L_COS + (32 * T4 + 16 + t16) * 8), s2 = g2 ? s1 : s0;
    auto hch = [&](const v4u& xv, u32 cb, u32 mu, u32 ru) {
      const float rs = __uint_as_float(ru), mr = -__uint_as_float(mu) * rs;
      v4u o;
#pragma unroll
      for (int z = 0; z < 2; ++z) {
        const v4f S = ldsr128f(cb + z * 16), A = ldsr128f(cb + 160 + z * 16);
        o[2 * z] = pkbf(__builtin_fmaf(__builtin_fmaf(bflo(xv[2 * z]), rs, mr), A[0], S[0]), __builtin_fmaf(__builtin_fmaf(bfhi(xv[2 * z]), rs, mr), A[1], S[1]));
        o[2 * z + 1] = pkbf(__builtin_fmaf(__builtin_fmaf(bflo(xv[2 * z + 1]), rs, mr), A[2], S[2]), __builtin_fmaf(__builtin_fmaf(bfhi(xv[2 * z + 1]), rs, mr), A[3], S[3]));
      }
      return o;
    };
    const u32 tb = WS_T + ((8 * P + 2 * T4) * 80 + 5 * J) * 256;
    stb128c(rs_ws, tb + vlin, hch(xs[0], cw + g4 * 32, s0[0], s0[1]));
    stb128c(rs_ws, tb + 20480 + vlin, hch(xs[1], cw + g4 * 32, s1[0], s1[1]));
    stb128c(rs_ws, lane < 32 ? tb + g2 * 20480 + 1024 + t16 * 16 : OOB, hch(xs[2], cw + 128, s2[0], s2[1]));
    drain();
    if (lane == 0) setflag(ws, WS_FLAG + (6 * 16 + P) * 512 + (T4 * 16 + J) * 4, tag);
  } else {
    {
      float tn[24];
#pragma unroll
      for (int z = 0; z < 3; ++z)
#pragma unroll
        for (int e = 0; e < 4; ++e) { tn[8 * z + 2 * e] = bflo(wm[z][e]); tn[8 * z + 2 * e + 1] = bfhi(wm[z][e]); }
      auto hsum = [](float a) { return xsum16(dppadd<DPP_ROR8>(dppadd<DPP_ROR4>(dppadd<0x4e>(dppadd<0xb1>(a))))); };
      float sm = 0.f, sm2 = 0.f;
#pragma unroll
      for (int e = 0; e < 24; ++e) { if (e < 16) sm += tn[e]; else sm2 += tn[e]; }
      const float m = hsum(i32 < 16 ? sm + sm2 : sm) * (1.f / DM);
      float q = 0.f, q2 = 0.f;
#pragma unroll
      for (int e = 0; e < 24; ++e) { const float d = tn[e] - m; if (e < 16) q += d * d; else q2 += d * d; }
      const float rs = __builtin_amdgcn_rsqf(hsum(i32 < 16 ? q + q2 : q) * (1.f / DM) + 1e-5f);
      stb64c(rs_ws, i32 < 2 ? WS_ST1 + (128 * P + 8 * J + 2 * T4 + hh) * 16 + i32 * 8 : OOB, v2u{__float_as_uint(i32 ? rs : m), tag});
    }
    bflag(rs_ws, tag, WS_FLAG + (5 * 16 + P) * 512 + 256 + (J * 4 + T4) * 4);
    waitv<2>();
    {
      const u32 ro = T4 * 1920;
      const v8bf f0 = trfrag(ta[0] + ro, ta[0] + ro + 960), f1 = trfrag(ta[1] + ro, ta[1] + ro + 960);
      const u32 tz = (T4 >> 1) ? ta[T4 & 1] : ta[0];
      const v8bf f2 = trfrag(tz + 7680, tz + 8640);
      stb128c(rs_ws, td[0][0], __builtin_bit_cast(v4u, f0)); stb128c(rs_ws, td[0][1], __builtin_bit_cast(v4u, f1));
      stb128c(rs_ws, (T4 >> 1) ? ((T4 & 1) ? td[1][1] : td[1][0]) : OOB, __builtin_bit_cast(v4u, f2));
    }
    drain();
    bflag(rs_ws, tag, WS_FLAG + (5 * 16 + J) * 512 + (P * 4 + T4) * 4);
    if (wave == 4) { poll_n(ws, WS_FLAG + (5 * 16 + J) * 512, 64, tag, lane); lds_post(L_SYNC + 8, 1u); }
    else {
      const rsrc_t rs_wo = mkrs(wo, DM * DM * 2);
      const int o0 = 2 * T4 - 2;
      const u32 so = (40 * P + 8 * o0) * 1280 + CT * 160, lr = L_OR + o0 * 1280;
      dmam<40>(rs_wo, v10, so, lr); dmam<40>(rs_wo, v10, so + 4 * 1280, lr + 640);
      if (T4 < 3) { dmam<40>(rs_wo, v10, so + 8 * 1280, lr + 1280); dmam<40>(rs_wo, v10, so + 12 * 1280, lr + 1920); }
      drain();
#pragma unroll 1
      for (int z = 0; z < 3; ++z) {
        const int ul = 4 * z + g4, hi = ul >= 5, tl = ul - 5 * hi, oct = 5 * P + o0 + hi;
        const bool ok = ul < (T4 < 3 ? 10 : 5);
        tile(lr + (ok ? hi * 1280 + tl * 32 : 0) + p4 * 8, 160, ok ? (oct >> 2) * 5120 + tl * 1024 + (oct & 3) * 256 + t16 * 16 : OOB, WS_WO + J * 102400);
      }
      lds_post(L_SYNC + 32 + wave * 4, 1u);
      lds_wait(L_SYNC + 8, 1u);
    }
#pragma unroll 1
    for (int e = T4; e < 100; e += 4) {
      if (e >= 88 && e < 92) lds_wait(L_SYNC + 52 + (lane % 3) * 4, 1u);
      dma16c(rs_ws, vlin, wqs + e * 1024, e * 1024);
    }
  }
  codepf();
  u32 ts0[4], ts1[4], tdd[4];
#pragma unroll
  for (int z = 0; z < 4; ++z) {
    const int k = wave + 8 * z;
    u32 src, rst, dst;
    if (k < 18) {
      const bool blk = k < 15;
      const int o5b = k / 3, bk = k - o5b * 3, o5 = blk ? o5b : 2 * (k - 15) + hh, oct = 5 * P + o5;
      src = L_R2 + (o5 < 5 ? o5 : 0) * 1792 + (blk ? hh * 8960 + bk * 64 + p4 * 16 + g2 * 8 : g2 * 8960 + 192 + p4 * 8); rst = 224;
      dst = WS_GU + J * 286720 + (oct & 3) * 256 + t16 * 16 + (blk ? (bk < 2 ? (oct >> 2) * 8192 + bk * 4096 : 163840 + (oct >> 2) * 4096) + g4 * 1024 : 245760 + (oct >> 2) * 2048 + g2 * 1024);
      dst = o5 < 5 ? dst : OOB;
    } else {
      const int kk = k < 28 ? k - 18 : 0, og = kk >= 5, tl = kk - 5 * og;
      src = L_RD + (4 * og + g4) * 1280 + tl * 32 + p4 * 8; rst = 160;
      dst = (k < 28 && rq27 < 27) ? WS_WD + CT * 276480 + (2 * rq27 + og) * 5120 + tl * 1024 + vlin : OOB;
    }
    ts0[z] = opq(src + q4 * rst); ts1[z] = opq(src + (4 + q4) * rst); tdd[z] = opq(dst);
  }
  if (wave == 0) poll_n(ws, WS_FLAG + (6 * 16 + P) * 512, 64, tag, lane);
  drain();
  bar();
  v4u hf[20];
  u32 lb[3];
  lb[0] = opq(vlin); lb[1] = opq(vlin + 51200); lb[2] = opq(vlin + 102400);
  auto frag = [&](u32 off) { return asbf(ldsr128(lb[off / 51200] + off % 51200)); };
  v4f aC[5] = {};
  v4u wpf[5][2];
  {
    constexpr int H0 = 8;
    const u32 hvo = WS_T + (8 * P + wave) * 20480 + vlin;
    auto ldh = [&](auto N) { aloadc<(N.v & 3) * 1024>(hf[N.v], rs_ws, hvo, (N.v >> 2) * 4096); };
    sfor<0, H0>(ldh);
  {
    const u32 mk0 = (q4 >> 1) == hh ? 0xffffffffu : 0u, mk1 = g2 ? 0u : mk0, sgn = (q4 & 1) ? 0x80008000u : 0u;
    const u32 r0 = L_WPHI + hh * 800 + q4 * 40;
#pragma unroll
    for (int T = 0; T < 5; ++T) {
      const u32 c = ((p4 & 1) ? (T + 1) % 5 : T) * 8;
      wpf[T][0] = __builtin_bit_cast(v4u, trfrag(r0 + g2 * 320 + c, r0 + g2 * 320 + 160 + c));
      const v2u l = __builtin_bit_cast(v2u, trd(r0 + 640 + c));
#pragma unroll
      for (int e = 0; e < 4; ++e) wpf[T][0][e] = (wpf[T][0][e] ^ sgn) & mk0;
      wpf[T][1] = v4u{(l[0] ^ sgn) & mk1, (l[1] ^ sgn) & mk1, 0u, 0u};
      asm volatile("" : "+v"(wpf[T][0]), "+v"(wpf[T][1]));
    }
  }
    v8bf wf[10], tfr;
    sfor<0, 10>([&](auto F) { wf[F.v] = frag(F.v * 1024); });
    SB();
    sfor<0, 20>([&](auto N) {
      constexpr int n = N.v;
      constexpr auto nst = [](int a, int b2) { int c = 0; for (int m = a < 0 ? 0 : a; m < b2; ++m) c += (m >= 2 && m < 10 && (m & 1) == 1) ? 1 : 0; return c; };
      if constexpr (n == 12) { rawbar(); SB(); }
      await1<7 + nst(n - 8, n)>(hf[n]);
      sfor<0, 5>([&](auto T) {
        constexpr int t = T.v, f = (5 * n + t) % 10;
        aC[t] = mfma16(wf[f], asbf(hf[n]), aC[t]);
        if constexpr (n < 18) wf[f] = frag((5 * n + 10 + t) * 1024);
        if constexpr (t == 0 && n < 12) ldh(IC<(n < 12 ? n + H0 : 0)>{});
        if constexpr (t == 2 && n >= 2 && n < 10) {
          constexpr int z = (n - 2) >> 1;
          if constexpr ((n & 1) == 0) tfr = trfrag(ts0[z], ts1[z]); else stb128c(rs_ws, tdd[z], __builtin_bit_cast(v4u, tfr));
        }
        if constexpr (t == 0 && n >= 12 && n < 19) {
          const int e = n < 18 ? 8 * (n - 12) + wave : 48 + (wave & 1);
          dma16c(rs_ws, vlin, wqs + 102400 + e * 1024, L_RQ + e * 1024);
        }
        SB();
      });
    });
    asm volatile("" : "+v"(aC[0]), "+v"(aC[1]), "+v"(aC[2]), "+v"(aC[3]), "+v"(aC[4]));
  }
  codepf();
  auto phi = [&](const v4f& m0, const v4f& m1, const v4f& l, u32 (&o)[10]) {
    const v4u b0 = {pkbf(m0[0], m0[1]), pkbf(m0[2], m0[3]), pkbf(m1[0], m1[1]), pkbf(m1[2], m1[3])};
    const v4u b1 = {pkbf(l[0], l[1]), pkbf(l[2], l[3]), 0u, 0u};
    float pv[20];
#pragma unroll
    for (int T = 0; T < 5; ++T) {
      v4f d = {};
      d = mfma16(asbf(wpf[T][0]), asbf(b0), d); d = mfma16(asbf(wpf[T][1]), asbf(b1), d);
#pragma unroll
      for (int e = 0; e < 4; ++e) pv[4 * T + e] = d[e];
    }
    softmax20(pv, o);
  };
  {
    u32 pk[10];
    phi(aC[0], aC[1], aC[4], pk);
    const int tok = 16 * wave + t16;
    const u32 fx = (((tok >> 1) & 1) << 2) | ((tok & 1) << 1) | ((tok >> 2) & 1), rb = hh * 16384 + tok * 128;
    auto cad = [&](int cc) { return rb + ((cc ^ fx) << 4); };
    bar();
    ldsw128(cad(g2), v4u{pkbf(aC[2][0], aC[2][1]), pkbf(aC[2][2], aC[2][3]), pkbf(aC[3][0], aC[3][1]), pkbf(aC[3][2], aC[3][3])});
    if (g2) ldsw128(cad(2), v4u{pkbf(aC[4][0], aC[4][1]), pkbf(aC[4][2], aC[4][3]), 0x00003f80u, 0u});
    ldsw128(cad(g2 ? 7 : 4), v4u{pk[0], pk[1], pk[2], pk[3]});
    ldsw128(cad(g2 ? 3 : 5), v4u{pk[4], pk[5], pk[6], pk[7]});
    ldsw64(cad(6) + g2 * 8, v2u{pk[8], pk[9]});
  }
  drain();
  bar();
  auto wflag = [&](int hop) { bflag(rs_ws, tag, WS_FLAG + (hop * 16 + P) * 512 + (wave * 16 + J) * 4); };
  if (wave < 4) {
    const int hd = wave >> 1, tl = wave & 1;
    u32 av[2], ap[2];
#pragma unroll
    for (int t = 0; t < 2; ++t) {
      const int tok = 8 * hh + 4 * t + q4;
      const u32 fx = (((tok >> 1) & 1) << 2) | ((tok & 1) << 1) | ((tok >> 2) & 1), cl = 2 * g2 + (p4 >> 1);
      av[t] = hd * 16384 + tok * 128 + ((cl ^ fx) << 4) + 8 * (p4 & 1);
      ap[t] = hd * 16384 + tok * 128 + ((((tl ? 0 : 4) + cl) ^ fx) << 4) + 8 * (p4 & 1);
    }
    v8bf fv[8], fp[8];
#pragma unroll
    for (int s = 0; s < 8; ++s) { fp[s] = trfrag(ap[0] + s * 2048, ap[1] + s * 2048); fv[s] = trfrag(av[0] + s * 2048, av[1] + s * 2048); }
    v16f d = {};
#pragma unroll
    for (int s = 0; s < 8; ++s) d = mfma32(fp[s], fv[s], d);
    const u32 base = i32 < 21 ? WS_KVP + (J * 16 + P) * KVP_STRIDE + hd * 1680 + i32 * 8 + hh * 168 : OOB;
    if (tl == 0) {
#pragma unroll
      for (int rq = 0; rq < 3; ++rq) stb64c(rs_ws, base + rq * 336, v2u{pkbf(d[4 * rq], d[4 * rq + 1]), pkbf(d[4 * rq + 2], d[4 * rq + 3])});
    }
    stb64c(rs_ws, base + (3 + tl) * 336, v2u{pkbf(d[12], d[13]), pkbf(d[14], d[15])});
    drain();
    if (lane == 0) setflag(ws, WS_FLAG + (0 * 16 + J) * 512 + (P * 4 + wave) * 4, tag);
  }
  const int bq = sopq(b);
  const bool dog2 = bq < 216;
  const int g2c = bq >> 2, g2k = bq & 3;
  v4u gw[5], gq[4], gr[4];
  const int ge = lane < 40 ? lane : 39, gvec = ge >= 20;
  const u32 gof = K2 ? OOB : WS_MODG + ((wave == 1 ? 1280 : wave == 2 ? 3200 : 2560) + CT * 80 + 2 * ge) * 8;
  const u32 gor = K2 ? OOB : WS_MODG + (1920 + gvec * 640 + 160 * g2k + 40 * T4) * 8 + (ge - 20 * gvec) * 16;
  if (K2) {
    const u32 so = (128 * P + 64 * TH + 16 * T4) * 1280 + CT * 160;
#pragma unroll
    for (int k = 0; k < 3; ++k) dmam<60>(rs_x, v10, so + 5 * k * 1280, 32768 + T4 * 2560 + k * 800);
  } else {
    const rsrc_t rs_gu = mkrs(g2c < 27 ? wg : wu, DM * FF * 2);
    const u32 vo = dog2 ? (160 * g2k + 40 * T4 + (lane >> 3)) * 3456 + (g2c < 27 ? g2c : g2c - 27) * 128 + (lane & 7) * 16 : OOB;
#pragma unroll
    for (int i = 0; i < 5; ++i) gw[i] = ldb128(rs_gu, vo, i * 8 * 3456);
#pragma unroll
    for (int i = 0; i < 4; ++i) { gr[i] = ldb128c(rs_ws, gor, i * 30720); gq[i] = ldb128c(rs_ws, gof, i * 30720); }
  }
  SB();
  auto wo1 = [&](int e) { dma16c(rs_ws, vlin, WS_WO + J * 102400 + e * 1024, e * 1024); };
  u32 pq[10];
  {
    v4f aQ[3] = {};
    const u32 bl = opq(L_RQ + 2048 + g4 * 128 + ((t16 >> 3) * 4 + p4) * 16);
    auto qf = [&](int n, int t) { return asbf(ldsr128(t == 2 ? bl + n * 2560 : lb[2] + n * 2560 + t * 1024)); };
    v8bf wf[9];
#pragma unroll
    for (int f = 0; f < 9; ++f) wf[f] = qf(f / 3, f % 3);
    SB();
#pragma unroll
    for (int n = 0; n < 60; ++n) {
      aQ[n % 3] = mfma16(wf[n % 9], asbf(hf[n / 3]), aQ[n % 3]);
      if (n + 9 < 60) wf[n % 9] = qf((n + 9) / 3, n % 3);
      SB();
    }
    asm volatile("" : "+v"(aQ[0]), "+v"(aQ[1]), "+v"(aQ[2]));
    phi(aQ[0], aQ[1], aQ[2], pq);
#pragma unroll
    for (int i = 0; i < 10; ++i) asm volatile("" : "+v"(pq[i]));
  }
  v2u xq[5];
  if (wave < 4) {
    if (!tags4(gr[0], gr[1], gr[2], gr[3], tag)) gather4(rs_ws, gor, 30720, tag, gr[0], gr[1], gr[2], gr[3]);
    if (!tags4(gq[0], gq[1], gq[2], gq[3], tag)) gather4(rs_ws, gof, 30720, tag, gq[0], gq[1], gq[2], gq[3]);
    *(LAS v2f*)(size_t)(L_G2A + T4 * 320 + ge * 8) = sum4(gr[0], gr[1], gr[2], gr[3], gvec ? 1.f : 0.f);
    if (wave) *(LAS v2f*)(size_t)(L_WPHI + (wave - 1) * 320 + ge * 8) = sum4(gq[0], gq[1], gq[2], gq[3], wave == 3 ? 1.f : 0.f);
    float sa[8] = {}, sb2[8] = {};
#pragma unroll
    for (int i = 0; i < 5; ++i) {
      const float cs = ldsrf(L_G2A + T4 * 320 + (8 * i + (lane >> 3)) * 4), ca = ldsrf(L_G2A + T4 * 320 + 160 + (8 * i + (lane >> 3)) * 4);
#pragma unroll
      for (int z = 0; z < 4; ++z) {
        const float lo = bflo(gw[i][z]), hi = bfhi(gw[i][z]);
        sa[2 * z] += ca * lo; sa[2 * z + 1] += ca * hi; sb2[2 * z] += cs * lo; sb2[2 * z + 1] += cs * hi;
      }
    }
    const u32 a = L_G2R + T4 * 512 + ((lane & 7) * 8 + 4 * hh + 2 * g2 + ((lane >> 3) & 1)) * 4;
    ldswf(a, red8(sa, lane)); ldswf(a + 256, red8(sb2, lane));
    lds_wait(L_SYNC + 48 + (lane & 3) * 4, 2u);
#pragma unroll 1
    for (int e = wave; e < WO_EARLY; e += 4) wo1(e);
  } else {
    drain();
#pragma unroll
    for (int t = 0; t < 5; ++t) { xq[t] = ldsr64(32768 + T4 * 2560 + t16 * 160 + t * 32 + g4 * 8); asm volatile("" : "+v"(xq[t])); }
    if (wave == 4) { poll_n(ws, WS_FLAG + (0 * 16 + J) * 512, 64, tag, lane); lds_post(L_SYNC + 28, 1u); } else lds_wait(L_SYNC + 28, 1u);
    const int t2 = tid - 256;
    v4u pv[16];
    const u32 c16 = t2 < 210 ? t2 * 16 : OOB;
#pragma unroll
    for (int i = 0; i < 16; ++i) pv[i] = ldb128c(rs_ws, c16, WS_KVP + (J * 16 + i) * KVP_STRIDE);
    lds_post(L_SYNC + 32 + wave * 4, 2u);
    u32 la[2];
#pragma unroll
    for (int z = 0; z < 2; ++z) { const int c = t2 < 210 ? 2 * t2 + z : 0, hd = c >= 210, rem = c - hd * 210, fq = rem / 21; la[z] = opq(L_KVB + hd * 1680 + (rem - fq * 21) * 80 + fq * 8); }
    SB();
    float sm[8] = {};
    const u32 clo = opq(0x00003f80u), chi = opq(0x3f800000u);
#pragma unroll
    for (int i = 0; i < 16; ++i)
#pragma unroll
      for (int e = 0; e < 4; ++e) { const u32 w = pv[i][e]; sm[2 * e] = dot2(w, clo, sm[2 * e]); sm[2 * e + 1] = dot2(w, chi, sm[2 * e + 1]); }
    if (t2 < 210) { ldsw64(la[0], v2u{pkbf(sm[0], sm[1]), pkbf(sm[2], sm[3])}); ldsw64(la[1], v2u{pkbf(sm[4], sm[5]), pkbf(sm[6], sm[7])}); }
  }
  bar();
  {
    const bool act = (t16 >> 3) == hh;
    const int hf2 = q4 & 1;
    const u32 kb = L_KVB + hh * 1680;
    const u32 a01 = kb + (4 * hh + 8 * hf2 + p4) * 80, a2 = kb + (hf2 ? 20 : (hh ? 0 : 16) + p4) * 80;
    v4f d[3] = {};
    const v4u b0 = {pq[0], pq[1], pq[2], pq[3]}, b1 = {pq[4], pq[5], pq[6], pq[7]}, b2 = {pq[8], pq[9], 0u, 0u};
#pragma unroll
    for (int T = 0; T < 3; ++T) {
      const u32 r = T < 2 ? a01 + T * 320 : a2;
      const v4u k0 = ldsr128(act ? r + g2 * 48 : L_ZERO), k1 = ldsr128(act ? r + 16 + g2 * 48 : L_ZERO);
      const v2u k2 = ldsr64(act ? r + 32 + g2 * 8 : L_ZERO);
      d[T] = mfma16(asbf(k0), asbf(b0), d[T]); d[T] = mfma16(asbf(k1), asbf(b1), d[T]); d[T] = mfma16(asbf(v4u{k2[0], k2[1], 0u, 0u}), asbf(b2), d[T]);
    }
    const auto dn = __builtin_amdgcn_permlane16_swap(__float_as_uint(d[2][0]), __float_as_uint(d[2][0]), false, false);
    const float rd = __builtin_amdgcn_rcpf(__uint_as_float(dn[1]) + 1e-6f);
    const u32 ob = WS_O + ((8 * P + wave) * 80 + 5 * J) * 256 + t16 * 16;
    stb128c(rs_ws, ob + (3 * hh + g2) * 256, v4u{pkbf(d[0][0] * rd, d[0][1] * rd), pkbf(d[0][2] * rd, d[0][3] * rd), pkbf(d[1][0] * rd, d[1][1] * rd), pkbf(d[1][2] * rd, d[1][3] * rd)});
    stb64c(rs_ws, g2 ? OOB : ob + 512 + hh * 8, v2u{pkbf(d[2][0] * rd, d[2][1] * rd), pkbf(d[2][2] * rd, d[2][3] * rd)});
  }
  drain();
  wflag(1);
  codepf();
  const u32 gus = WS_GU + J * 286720;
  const int tokE = 128 * P + 64 * TH + 16 * T4 + t16;
  if (dog2 && wave == 7) {
    const u32 a = L_G2R + lane * 4;
    const float av = (ldsrf(a) + ldsrf(a + 512)) + (ldsrf(a + 1024) + ldsrf(a + 1536));
    const float bv = (ldsrf(a + 256) + ldsrf(a + 768)) + (ldsrf(a + 1280) + ldsrf(a + 1792));
    stb64c(rs_ws, WS_AB + (g2k * 3456 + g2c * 64 + lane) * 16, v2u{__float_as_uint(av), tag});
    stb64c(rs_ws, WS_AB + (g2k * 3456 + g2c * 64 + lane) * 16 + 8, v2u{__float_as_uint(bv), tag});
  }
  if (wave >= 1) {
#pragma unroll 1
    for (int e = WO_EARLY + wave - 1; e < 100; e += 7) wo1(e);
    drain();
  }
  SB();
  if (wave == 0) poll_n(ws, WS_FLAG + (1 * 16 + P) * 512 + TH * 256, 64, tag, lane);
  bar();
  auto gu1 = [&](int e8) {
    dma16c(rs_ws, vlin, gus + (e8 + wave) * 1024, ((e8 + 104) % 144 + wave) * 1024);
  };
  u32 kb2[3];
  kb2[0] = opq(vlin + K2 * 5120); kb2[1] = opq(vlin + K2 * 5120 + 51200); kb2[2] = opq(vlin + K2 * 5120 + 102400);
  auto frag2 = [&](u32 off) { return asbf(ldsr128(kb2[off / 51200] + off % 51200)); };
  float x1[20];
  {
    v4u of[10];
    const u32 ab = WS_O + ((8 * P + 4 * TH + T4) * 80 + 4 * K2) * 256 + vlin;
    sfor<0, 10>([&](auto S) { aloadc<(S.v & 1) * 2048>(of[S.v], rs_ws, ab, (S.v >> 1) * 4096); });
    sfor<0, 5>([&](auto I) { gu1(8 * I.v); });
    v8bf wf[10];
    sfor<0, 10>([&](auto F) { wf[F.v] = frag2((F.v / 5) * 10240 + (F.v % 5) * 1024); });
    v4f acc[5] = {};
    SB();
    sfor<0, 10>([&](auto S) {
      constexpr int s = S.v;
      await1<14 - s>(of[s]);
      sfor<0, 5>([&](auto T) {
        constexpr int t = T.v, f = (5 * s + t) % 10;
        acc[t] = mfma16(wf[f], asbf(of[s]), acc[t]);
        if constexpr (s < 8) wf[f] = frag2((s + 2) * 10240 + t * 1024);
        SB();
      });
    });
    asm volatile("" : "+v"(acc[0]), "+v"(acc[1]), "+v"(acc[2]), "+v"(acc[3]), "+v"(acc[4]));
    bar();
    const u32 xb = 81920 + T4 * 5120 + vlin;
    if (K2 == 0) {
#pragma unroll
      for (int t = 0; t < 5; ++t) ldsw128f(xb + t * 1024, acc[t]);
    }
    v4f ga[5], A2[5];
#pragma unroll
    for (int t = 0; t < 5; ++t) { ga[t] = ldsr128f(L_WPHI + t * 64 + g4 * 16); A2[t] = ldsr128f(L_WPHI + 640 + t * 64 + g4 * 16); asm volatile("" : "+v"(ga[t]), "+v"(A2[t])); }
    const u32 uo = opq(WS_U + ((tokE >> 4) * 80 + CT * 10 + hh) * 256 + t16 * 16 + g2 * 8);
    bar();
    if (K2 == 1) {
      v4f s1 = {}, s2 = {};
#pragma unroll
      for (int t = 0; t < 5; ++t) {
        const v4f y = v4f{bflo(xq[t][0]), bfhi(xq[t][0]), bflo(xq[t][1]), bfhi(xq[t][1])} + ga[t] * (acc[t] + ldsr128f(xb + t * 1024));
        const u32 p0 = pkbf(y[0], y[1]), p1 = pkbf(y[2], y[3]);
        const v4f r = {bflo(p0), bfhi(p0), bflo(p1), bfhi(p1)}, u = r * A2[t];
        stb64c(rs_ws, uo + t * 512, v2u{pkbf(u[0], u[1]), pkbf(u[2], u[3])});
        x1[4 * t] = r[0]; x1[4 * t + 1] = r[1]; x1[4 * t + 2] = r[2]; x1[4 * t + 3] = r[3];
        s1 += r;
      }
      const float mt = xsum32(xsum16((s1[0] + s1[1]) + (s1[2] + s1[3]))) * (1.f / 80.f);
#pragma unroll
      for (int t = 0; t < 5; ++t) { const v4f d = v4f{x1[4 * t], x1[4 * t + 1], x1[4 * t + 2], x1[4 * t + 3]} - mt; s2 += d * d; }
      const float m2 = xsum32(xsum16((s2[0] + s2[1]) + (s2[2] + s2[3])));
      stb64c(rs_ws, g4 == 0 ? WS_STAT + ((P * 8 + CT) * 128 + (tokE & 127)) * 8 : OOB, v2u{__float_as_uint(mt), __float_as_uint(m2)});
      drain();
      if (lane == 0) setflag(ws, WS_FLAG + (2 * 16 + P) * 512 + (J * 4 + T4) * 4, tag);
#pragma unroll 1
      for (int z = 0; z < 6; ++z) { const int e = z < 5 ? 120 + 5 * T4 + z : 140 + T4; dma16c(rs_ws, vlin, gus + e * 1024, (e - 40) * 1024); }
    } else {
#pragma unroll
      for (int i = 0; i < 20; ++i) x1[i] = 0.f;
      {
        const int abe = T4 * 56 + (lane < 56 ? lane : 55), mat = abe >= 112, c = abe - 112 * mat;
        v4u g0, g1, g2v, g3;
        gather4(rs_ws, WS_AB + (mat * 1728 + (t0 * 16 + c < 1728 ? t0 * 16 + c : 1727)) * 16, 3456 * 16, tag, g0, g1, g2v, g3);
        ldswf(L_AB + abe * 4, (__uint_as_float(g0[0]) + __uint_as_float(g1[0])) + (__uint_as_float(g2v[0]) + __uint_as_float(g3[0])));
        ldswf(L_AB + 896 + abe * 4, (__uint_as_float(g0[2]) + __uint_as_float(g1[2])) + (__uint_as_float(g2v[2]) + __uint_as_float(g3[2])));
      }
      if (wave > 0) {
#pragma unroll 1
        for (int e = 39 + wave; e < 120; e += 3) { dma16c(rs_ws, vlin, gus + e * 1024, (e - 40) * 1024); waitv<GU_THR>(); }
      }
    }
  }
  codepf();
  const u32 wds = WS_WD + CT * 276480;
  v4u af[27];
  const u32 ab4 = WS_ACT + ((8 * P + 4 * TH + T4) * 216 + 4 * K2) * 256 + vlin;
  auto lda = [&](auto N) { constexpr int n = N.v; aloadc<(n & 1) * 2048>(af[n], rs_ws, ab4, (n >> 1) * 4096); };
  u32 pk1;
  const u32 pko = wave == 7 ? WS_FLAG + P * 512 + TH * 256 + lane * 4 : OOB;
  auto peek = [&](int hop) { asm volatile("buffer_load_dword %0, %1, %2, %3 offen sc1" : "=v"(pk1) : "v"(pko), "s"(rs_ws), "s"(hop * 8192) : "memory"); };
  auto check = [&](auto N, int hop) {
    asm volatile("s_waitcnt vmcnt(%1)" : "+v"(pk1) : "i"(N.v) : "memory");
    if (wave == 7 && !__all(pk1 == tag)) poll_n(ws, WS_FLAG + (hop * 16 + P) * 512 + TH * 256, 64, tag, lane);
  };
  auto wd1 = [&](int e) { dma16c(rs_ws, vlin, wds + e * 1024, wrap(wrap(e + 100, 150), 150) * 1024); };
  {
    if (wave == 0) poll_n(ws, WS_FLAG + (2 * 16 + P) * 512, 64, tag, lane);
    bar();
      v4u uf[20], st;
    const u32 uvo = WS_U + (8 * P + wave) * 20480 + vlin;
    constexpr int U0 = 8;
    auto ldu = [&](auto N) { aloadc<(N.v & 3) * 1024>(uf[N.v], rs_ws, uvo, (N.v >> 2) * 4096); };
    sfor<0, U0>(ldu);
    const u32 stoff = WS_STAT + ((P * 8 + 2 * g4 + (lane & 1)) * 128 + 16 * wave + (t16 & ~1)) * 8;
    constexpr auto sl = [](int e) { return (u32)(((e + 104) % 144) * 1024); };
    v8bf wf[8];
    sfor<0, 8>([&](auto F) { wf[F.v] = frag(sl(F.v)); });
    v4f aA[8] = {}, aB[6] = {};
    constexpr auto nops = [](int m) { return (m < 13 ? 1 : 0) + (m >= 8 ? 1 : 0); };
    constexpr auto sum = [=](int a, int b2) { int c = 0; for (int m = a; m < b2; ++m) c += nops(m); return c; };
    SB();
    sfor<0, 20>([&](auto N) {
      constexpr int n = N.v;
      if constexpr (n == 2 || n == 8 || n == 13) { rawbar(); SB(); }
      if constexpr (n == 17) { await1<3>(st); rawbar(); SB(); }
      await1<(n < U0 ? U0 - 1 - n + sum(0, n) : nops(n - U0) - 1 + sum(n - U0 + 1, n))>(uf[n]);
      sfor<0, 8>([&](auto T) {
        constexpr int t = T.v;
        aA[t] = mfma16(wf[t], asbf(uf[n]), aA[t]);
        wf[t] = frag(sl(8 * n + 8 + t));
        if constexpr (t == 0) {
          if constexpr (n + U0 < 20) ldu(IC<(n + U0 < 20 ? n + U0 : 0)>{});
          else if constexpr (n == 12) aloadc<0>(st, rs_ws, stoff, 0);
        }
        if constexpr (t == 4 && n >= 8) gu1(144 + 8 * (n - 8));
        SB();
      });
    });
    asm volatile("" : "+v"(aA[0]), "+v"(aA[1]), "+v"(aA[2]), "+v"(aA[3]), "+v"(aA[4]), "+v"(aA[5]), "+v"(aA[6]), "+v"(aA[7]));
  codepf();
    float rs2, mr2;
    {
      const bool od = lane & 1;
      const float ma = __uint_as_float(od ? st[2] : st[0]), qa = __uint_as_float(od ? st[3] : st[1]);
      const float mb = __uint_as_float(__builtin_amdgcn_update_dpp(0u, od ? st[0] : st[2], 0xb1, 0xf, 0xf, true));
      const float qb = __uint_as_float(__builtin_amdgcn_update_dpp(0u, od ? st[1] : st[3], 0xb1, 0xf, 0xf, true));
      const float m = xsum32(xsum16(ma + mb)) * 0.125f;
      const float q = qa + qb + 80.f * ((ma - m) * (ma - m) + (mb - m) * (mb - m));
      rs2 = __builtin_amdgcn_rsqf(xsum32(xsum16(q)) * (1.f / DM) + 1e-5f); mr2 = -m * rs2;
    }
    auto geglu4 = [&](const v4f& g, const v4f& u, u32 cb) {
      const v4f ag = ldsr128f(cb), au = ldsr128f(cb + 448), bg = ldsr128f(cb + 896), bu = ldsr128f(cb + 1344);
      const v4f gv = g * rs2 + (ag * mr2 + bg), uv = u * rs2 + (au * mr2 + bu);
      const v4f z = gv * (gv * gv * (-0.044715f * 2.302208198f) + (-2.302208198f));
      v4f d = {__builtin_amdgcn_exp2f(z[0]), __builtin_amdgcn_exp2f(z[1]), __builtin_amdgcn_exp2f(z[2]), __builtin_amdgcn_exp2f(z[3])};
      d += 1.f;
      const v4f r = {__builtin_amdgcn_rcpf(d[0]), __builtin_amdgcn_rcpf(d[1]), __builtin_amdgcn_rcpf(d[2]), __builtin_amdgcn_rcpf(d[3])};
      const v4f o = gv * uv * r;
      return v2u{pkbf(o[0], o[1]), pkbf(o[2], o[3])};
    };
    const u32 abm = opq(L_AB + g4 * 32);
    const u32 ob = WS_ACT + (8 * P + wave) * 55296;
    auto epiA = [&]() {
#pragma unroll
      for (int bk = 0; bk < 2; ++bk) {
        const v2u lo = geglu4(aA[4 * bk], aA[4 * bk + 2], abm + bk * 128), hi = geglu4(aA[4 * bk + 1], aA[4 * bk + 3], abm + bk * 128 + 16);
        astorec(v4u{lo[0], lo[1], hi[0], hi[1]}, rs_ws, vlin, ob + (2 * J + bk) * 1024);
      }
    };
    auto loopB = [&](auto I0, auto I1) {
      sfor<I0.v, I1.v>([&](auto I) {
        constexpr int i = I.v, a = i < 80 ? i % 4 : 4 + i % 2, st = i < 80 ? i / 4 : (i - 80) / 2;
        aB[a] = mfma16(wf[i % 8], asbf(uf[st]), aB[a]);
        if constexpr (i + 8 < 120) wf[i % 8] = frag(sl(160 + i + 8));
        if constexpr (i < 30 && i % 6 == 3) gu1(240 + 8 * (i / 6));
        if constexpr (i == 33 || i == 39) wd1(8 * ((i - 33) / 6) + wave);
        if constexpr (i == 45) wd1(16 + (wave & 3));
        if constexpr (i >= 60 && i < 80 && i % 5 != 0 && i % 5 != 3 && (i % 5 == 1 || i % 5 == 4)) wd1(20 + 8 * (2 * ((i - 60) / 5) + (i % 5 == 4)) + wave);
        if constexpr (i >= 80 && i < 112 && i % 4 == 1) lda(IC<(i >= 80 ? (i - 80) / 4 : 0)>{});
        if constexpr (i == 83 || i == 91) wd1(84 + (i - 83) + wave);
        SB();
      });
    };
    auto pinB = [&]() { asm volatile("" : "+v"(aB[0]), "+v"(aB[1]), "+v"(aB[2]), "+v"(aB[3]), "+v"(aB[4]), "+v"(aB[5])); };
    if (!K2) epiA();
    SB();
    loopB(IC<0>{}, IC<24>{});
    pinB();
    if (K2) epiA();
    SB();
    waitv<6>();
    rawbar();
    loopB(IC<24>{}, IC<44>{});
    waitv<3>();
    wflag(3);
    loopB(IC<44>{}, IC<60>{});
    waitv<4>();
    rawbar();
    loopB(IC<60>{}, IC<80>{});
    pinB();
    peek(3);
    {
      const v2u lo = geglu4(aB[0], aB[2], abm + 256), hi = geglu4(aB[1], aB[3], abm + 272);
      astorec(v4u{lo[0], lo[1], hi[0], hi[1]}, rs_ws, vlin, ob + (32 + J) * 1024);
    }
    check(IC<1>{}, 3);
    rawbar();
  codepf();
    loopB(IC<80>{}, IC<120>{});
    pinB();
    waitv<10>();
    wflag(4);
    {
      const v2u hf4 = geglu4(aB[4], aB[5], L_AB + 384 + g4 * 16);
      __builtin_amdgcn_raw_buffer_store_b64(hf4, rs_ws, nt == 7 ? hh * 256 + t16 * 16 + g2 * 8 : OOB, ob + 48 * 1024 + (J - (J >> 2)) * 512, AUX_SC1);
    }
  }
  rawbar();
  {
    constexpr auto sl = [](int n, int t) { return (u32)(((n + 10) % 15) * 10240 + t * 1024); };
    v8bf wf[10];
    sfor<0, 10>([&](auto F) { wf[F.v] = frag2(sl(F.v / 5, F.v % 5)); });
    v4f acc[5] = {};
    constexpr auto nw = [](int m) { return m < 3 ? 2 : m < 19 ? 1 : 0; };
    constexpr auto nops = [=](int m) { return (m < 19 ? 1 : 0) + nw(m) + ((m == 3 || m == 5 || m == 13) ? 1 : 0); };
    constexpr auto sum = [=](int a, int b2) { int c = 0; for (int m = a; m < b2; ++m) c += nops(m); return c; };
    SB();
    sfor<0, 27>([&](auto N) {
      constexpr int n = N.v;
      if constexpr (n == 3) { waitv<sum(0, 3)>(); wflag(7); }
      if constexpr (n == 5) peek(4);
      if constexpr (n == 13) peek(7);
      if constexpr (n == 8) check(IC<sum(5, 8) - 1>{}, 4);
      if constexpr (n == 16) check(IC<sum(13, 16) - 1>{}, 7);
      if constexpr (n >= 2 && n <= 24 && (n & 1) == 0) { if constexpr (n >= 6) waitv<sum(n - 4, n)>(); rawbar(); SB(); }
      if constexpr (n < 8) await1<(7 - n) + (n < 1 ? 2 : n < 3 ? 1 : 0) + 2 + sum(0, n) + ((n == 3 || n == 5) ? 1 : 0)>(af[n]);
      else await1<nw(n - 8) + sum(n - 7, n) + (n == 13 ? 1 : 0)>(af[n]);
      sfor<0, 5>([&](auto T) {
        constexpr int t = T.v, f = (5 * n + t) % 10;
        acc[t] = mfma16(wf[f], asbf(af[n]), acc[t]);
        if constexpr (n < 25) wf[f] = frag2(sl(n + 2, t));
        if constexpr (n < 19 && t == 0) lda(IC<(n < 19 ? n + 8 : 0)>{});
        if constexpr (n < 3 && (t == 2 || t == 4)) wd1(100 + 16 * n + 4 * (t - 2) + wave);
        if constexpr (n >= 3 && n < 19 && t == 2) { const int e = 148 + 8 * (n - 3) + wave; wd1(n == 18 ? (e < 270 ? e : 269) : e); }
        SB();
      });
      if constexpr (n == 7 || n == 15) asm volatile("" : "+v"(acc[0]), "+v"(acc[1]), "+v"(acc[2]), "+v"(acc[3]), "+v"(acc[4]));
    });
    asm volatile("" : "+v"(acc[0]), "+v"(acc[1]), "+v"(acc[2]), "+v"(acc[3]), "+v"(acc[4]));
    const u32 xb = 81920 + T4 * 5120 + vlin;
    if (K2 == 0) {
#pragma unroll
      for (int t = 0; t < 5; ++t) ldsw128f(xb + t * 1024, acc[t]);
    }
    bar();
    if (K2 == 1) {
      const u32 ob = 102400 + T4 * 2816;
#pragma unroll
      for (int t = 0; t < 5; ++t) {
        const v4f a = acc[t] + ldsr128f(xb + t * 1024);
        const v4f gb = ldsr128f(L_WPHI + 320 + t * 64 + g4 * 16);
        ldsw64(ob + t16 * 176 + t * 32 + g4 * 8, v2u{pkbf(x1[4 * t] + gb[0] * a[0], x1[4 * t + 1] + gb[1] * a[1]), pkbf(x1[4 * t + 2] + gb[2] * a[2], x1[4 * t + 3] + gb[3] * a[3])});
      }
#pragma unroll
      for (int i = 0; i < 3; ++i) {
        const int it = i * 64 + lane, tk = it / 10, ch = it - tk * 10;
        if (it < 160) *(GAS v4u*)((GAS char*)out + (128 * P + 64 * TH + 16 * T4 + tk) * 1280 + CT * 160 + ch * 16) = ldsr128(ob + tk * 176 + ch * 16);
      }
    }
  }
  if (b == 0 && tid == 0) __hip_atomic_store((GAS u32*)(ws + WS_EPOCH), tag, RLX_AGENT);
}
}

extern "C" void kernel_launch(void* const* d_in, const int* in_sizes, int n_in, void* d_out, int out_size, void* d_ws, size_t ws_size, hipStream_t stream) {
  (void)in_sizes; (void)n_in; (void)out_size; (void)ws_size;
  (void)hipFuncSetAttribute((const void*)fused_kernel, hipFuncAttributeMaxDynamicSharedMemorySize, L_TOTAL);
  fused_kernel<<<256, 512, L_TOTAL, stream>>>((const u16*)d_in[0], (const u16*)d_in[1], (const u16*)d_in[2], (const u16*)d_in[3], (const u16*)d_in[5],
                                              (const u16*)d_in[6], (const u16*)d_in[7], (const u16*)d_in[8], (const u16*)d_in[9], (u16*)d_out, (char*)d_ws);
}
```
